```python
import torch

def _softmax(x, axis=-1):
    return torch.softmax(x, dim=axis)
def _gelu(x):
    return torch.nn.functional.gelu(x, approximate="tanh")
def _silu(x):
    return torch.nn.functional.silu(x)
def _sigmoid(x):
    return torch.sigmoid(x)
def _layernorm(x, axis=-1, eps=1e-5):
    m = x.mean(dim=axis, keepdim=True)
    v = x.var(dim=axis, keepdim=True, unbiased=False)
    return (x - m) / torch.sqrt(v + eps)
def _scatter_add(tgt, idx, src, axis=0):
    return tgt.clone().index_add_(axis, idx, src)
def _segment_sum(src, seg, n):
    out = torch.zeros((n,) + src.shape[1:], dtype=src.dtype)
    return out.index_add_(0, seg, src)
def _pad(x, pw):
    flat = tuple(v for pair in reversed(pw) for v in pair)
    return torch.nn.functional.pad(x, flat)

kernel_name = 'parallel_sum__ssm_conv_h3__glu_gated_glu__prompt'

def setup_inputs():

    _w = (lambda a: (lambda t: t.to(torch.bfloat16) if t.is_floating_point() else t)(torch.as_tensor(a)))
    def _A_setup():
        B, S, D = 1, 2048, 1024
        ks = 4
        return {
            "q": _w((lambda *s: torch.randn(*s, dtype=torch.bfloat16) * 0.05)(B, S, D)),
            "k": _w((lambda *s: torch.randn(*s, dtype=torch.bfloat16) * 0.8)(B, S, D)),
            "v": _w((lambda *s: torch.randn(*s, dtype=torch.bfloat16) * 0.8)(B, S, D)),
            "shift_k": _w((lambda *s: torch.randn(*s, dtype=torch.bfloat16) * 0.8)(D, ks) * 0.5),
            "ssm_k": _w((lambda *s: torch.randn(*s, dtype=torch.bfloat16) * 0.8)(D, S) * (1.0 / S ** 0.5)),
            "ssm_D": _w((lambda *s: torch.randn(*s, dtype=torch.bfloat16) * 0.8)(D)),
        }
    _A_in = _A_setup()

    def _B_setup():
        B, S, D, F = 1, 2048, 1024, 3584
        return {
            "x": _w((lambda *s: torch.randn(*s, dtype=torch.bfloat16) * 0.05)(B, S, D)),
            "w_gate": _w((lambda *s: torch.randn(*s, dtype=torch.bfloat16) * 0.05)(D, F)),
            "w_up": _w((lambda *s: torch.randn(*s, dtype=torch.bfloat16) * 0.05)(D, F)),
            "w_down": _w((lambda *s: torch.randn(*s, dtype=torch.bfloat16) * 0.05)(F, D)),
        }
    _B_in = _B_setup()

    return {
        "A_q": _A_in["q"],
        "A_k": _A_in["k"],
        "A_v": _A_in["v"],
        "A_shift_k": _A_in["shift_k"],
        "A_ssm_k": _A_in["ssm_k"],
        "A_ssm_D": _A_in["ssm_D"],
        "B_x": _B_in["x"],
        "B_w_gate": _B_in["w_gate"],
        "B_w_up": _B_in["w_up"],
        "B_w_down": _B_in["w_down"],
    }

def reference(A_q, A_k, A_v, A_shift_k, A_ssm_k, A_ssm_D, B_x, B_w_gate, B_w_up, B_w_down):
    def _A_ref(q, k, v, shift_k, ssm_k, ssm_D):
        B, S, D = q.shape
        ks = shift_k.shape[-1]
        k_pad = _pad(k, ((0, 0), (ks - 1, 0), (0, 0)))
        k_shift = torch.zeros_like(k)
        for t in range(ks):
            k_shift = k_shift + k_pad[:, t:t + S, :] * shift_k[:, ks - 1 - t][None, None, :]
        kv = k_shift * v
        L = 2 * S
        kv_pad = _pad(kv, ((0, 0), (0, S), (0, 0)))
        kf_pad = _pad(ssm_k, ((0, 0), (0, S)))
        kv_f = (lambda x, axes=(-2,-1): torch.fft.rfft2(x.float(), dim=axes))(kv_pad[:, None, :, :], axes=(1, 2))
        kf_f = (lambda x, axes=(-2,-1): torch.fft.rfft2(x.float(), dim=axes))(kf_pad[:, None, :], axes=(1, 2))
        kf_f = (lambda x, *d: x.permute(*d))(kf_f, 1, 2, 0)
        y_f = kv_f * kf_f[None, :, :, :]
        y = (lambda x, s, axes=(-2,-1): torch.fft.irfft2(x, s=s, dim=axes).to(torch.bfloat16))(y_f, (1, L), axes=(1, 2))[:, 0, :S, :]
        y = y + kv * ssm_D[None, None, :]
        return q * y

    def _B_ref(x, w_gate, w_up, w_down):
        g = torch.einsum("bsd,df->bsf", x, w_gate)
        u = torch.einsum("bsd,df->bsf", x, w_up)
        h = _sigmoid(g) * u
        return torch.einsum("bsf,fd->bsd", h, w_down)

    x = A_q
    xn = _layernorm(x, axis=-1)
    return x + _A_ref(xn, A_k, A_v, A_shift_k, A_ssm_k, A_ssm_D) + _B_ref(xn, B_w_gate, B_w_up, B_w_down)
```

```cpp
#include <hip/hip_runtime.h>
#include <hip/hip_bf16.h>
#include <stdint.h>

#define LAS __attribute__((address_space(3)))
#define GAS __attribute__((address_space(1)))
typedef unsigned short u16;
typedef short bf16x8 __attribute__((ext_vector_type(8)));
typedef short bf16x4 __attribute__((ext_vector_type(4)));
typedef float f32x4 __attribute__((ext_vector_type(4)));
typedef float f32x2 __attribute__((ext_vector_type(2)));
typedef unsigned u32x4 __attribute__((ext_vector_type(4)));
typedef unsigned u32x2 __attribute__((ext_vector_type(2)));
typedef __bf16 bf16x2_t __attribute__((ext_vector_type(2)));

constexpr int SEQ = 2048, DM = 1024, FF = 3584;

__device__ __forceinline__ unsigned pack_bf16(float lo, float hi) {
    f32x2 v = {lo, hi};
    return __builtin_bit_cast(unsigned, __builtin_convertvector(v, bf16x2_t));
}
__device__ __forceinline__ float bf_lo(unsigned u) { return __uint_as_float(u << 16); }
__device__ __forceinline__ float bf_hi(unsigned u) { return __uint_as_float(u & 0xffff0000u); }
__device__ __forceinline__ float bf_round(float f) { return bf_lo(pack_bf16(f, f)); }

#define WAIT_V(n) asm volatile("s_waitcnt vmcnt(" #n ")" ::: "memory")
#define WAIT_L(n) asm volatile("s_waitcnt lgkmcnt(" #n ")" ::: "memory")
#define BAR() __builtin_amdgcn_s_barrier()
#define SCHED() __builtin_amdgcn_sched_barrier(0)
#define GLDS(gptr, ldsptr) __builtin_amdgcn_global_load_lds((const GAS unsigned*)(gptr), (LAS unsigned*)(ldsptr), 16, 0, 0)

#define TRRD(dst, addr, off) asm volatile("ds_read_b64_tr_b16 %0, %1 offset:%c2" : "=&v"(dst) : "v"(addr), "i"(off) : "memory")
#define CAT(lo, hi) __builtin_shufflevector(lo, hi, 0, 1, 2, 3, 4, 5, 6, 7)

__global__ __launch_bounds__(256) void k1_ln(const u16* __restrict__ x, u16* __restrict__ xn, f32x2* __restrict__ stats) {
    const int row = blockIdx.x * 4 + (threadIdx.x >> 6), lane = threadIdx.x & 63;
    const u32x4* p = (const u32x4*)(x + (size_t)row * DM);
    const u32x4 a = p[lane], b = p[lane + 64];
    float v[16];
#pragma unroll
    for (int i = 0; i < 4; ++i) { v[2 * i] = bf_lo(a[i]); v[2 * i + 1] = bf_hi(a[i]); v[8 + 2 * i] = bf_lo(b[i]); v[9 + 2 * i] = bf_hi(b[i]); }
    float s = 0.f;
#pragma unroll
    for (int i = 0; i < 16; ++i) s += v[i];
#pragma unroll
    for (int o = 32; o >= 1; o >>= 1) s += __shfl_xor(s, o);
    const float mean = s * (1.0f / DM);
    float q = 0.f;
#pragma unroll
    for (int i = 0; i < 16; ++i) { const float d = v[i] - mean; q += d * d; }
#pragma unroll
    for (int o = 32; o >= 1; o >>= 1) q += __shfl_xor(q, o);
    const float mb = bf_round(mean), vb = bf_round(q * (1.0f / DM));
    const float sb = bf_round(sqrtf(bf_round(vb + 1e-5f)));
    const float rs = 1.0f / sb;
    u32x4 oa, ob;
#pragma unroll
    for (int i = 0; i < 4; ++i) {
        oa[i] = pack_bf16(bf_round(v[2 * i] - mb) * rs, bf_round(v[2 * i + 1] - mb) * rs);
        ob[i] = pack_bf16(bf_round(v[8 + 2 * i] - mb) * rs, bf_round(v[9 + 2 * i] - mb) * rs);
    }
    u32x4* o = (u32x4*)(xn + (size_t)row * DM);
    o[lane] = oa; o[lane + 64] = ob;
    if (lane == 0) stats[row] = (f32x2){mb, rs};
}

constexpr int CV_XB = 0, CV_XB0 = 512, CV_HE = 4608, CV_HO = 4608 + 4160, CV_WAVE = 4608 + 2 * 4160;

__device__ __forceinline__ void conv_block(LAS unsigned char* lds, int cb, const u16* __restrict__ kin, const u16* __restrict__ vin, const u16* __restrict__ shk,
                                           const u16* __restrict__ ssmk, const u16* __restrict__ ssmD, u16* __restrict__ kvT, u16* __restrict__ y2T) {
    const int tid = threadIdx.x, c0 = cb * 32;
    {
        const int cg = tid & 3, run = tid >> 2, ch = c0 + 8 * cg, r0 = 16 * run;
        u32x4 kr[19], vr[16], wr[4];
#pragma unroll
        for (int i = 0; i < 19; ++i) {
            const int r = r0 - 3 + i;
            kr[i] = *(const u32x4*)(kin + (size_t)(r < 0 ? 0 : r) * DM + ch);
        }
#pragma unroll
        for (int i = 0; i < 16; ++i) vr[i] = *(const u32x4*)(vin + (size_t)(r0 + i) * DM + ch);
#pragma unroll
        for (int i = 0; i < 4; ++i) wr[i] = *(const u32x4*)(shk + (size_t)ch * 4 + 8 * i);
        if (run == 0) {
#pragma unroll
            for (int i = 0; i < 3; ++i) kr[i] = (u32x4){0u, 0u, 0u, 0u};
        }
#pragma unroll
        for (int c = 0; c < 8; ++c) {
            const unsigned w01 = wr[c >> 1][2 * (c & 1)], w23 = wr[c >> 1][2 * (c & 1) + 1];
            const float w0 = bf_lo(w01), w1 = bf_hi(w01), w2 = bf_lo(w23), w3 = bf_hi(w23);
            float kf[19], kv[16];
#pragma unroll
            for (int i = 0; i < 19; ++i) kf[i] = (c & 1) ? bf_hi(kr[i][c >> 1]) : bf_lo(kr[i][c >> 1]);
#pragma unroll
            for (int i = 0; i < 16; ++i) {
                const float vf = (c & 1) ? bf_hi(vr[i][c >> 1]) : bf_lo(vr[i][c >> 1]);
                const float ks = kf[i] * w3 + kf[i + 1] * w2 + kf[i + 2] * w1 + kf[i + 3] * w0;
                kv[i] = ks * vf;
            }
            u32x4 o0, o1;
#pragma unroll
            for (int i = 0; i < 4; ++i) { o0[i] = pack_bf16(kv[7 - 2 * i], kv[6 - 2 * i]); o1[i] = pack_bf16(kv[15 - 2 * i], kv[14 - 2 * i]); }
            u32x4* dst = (u32x4*)(kvT + (size_t)(ch + c) * SEQ + r0);
            dst[0] = o0; dst[1] = o1;
        }
    }
    const int wid = __builtin_amdgcn_readfirstlane(tid >> 6), lane = tid & 63;
    LAS unsigned char* wl = lds + wid * CV_WAVE;
    if (lane < 32) *(LAS u32x4*)(wl + CV_XB + lane * 16) = (u32x4){0u, 0u, 0u, 0u};
    if (lane < 2) { *(LAS u32x4*)(wl + CV_HE + lane * 16) = (u32x4){0u, 0u, 0u, 0u}; *(LAS u32x4*)(wl + CV_HO + lane * 16) = (u32x4){0u, 0u, 0u, 0u}; }
    __syncthreads();
    const int r = lane & 15, g = lane >> 4, g0 = g & 1, gh = g >> 1;
    const int s0 = r - 8 * g0 + 9 + 16 * gh;
    const int aoff = (r & 1) ? (CV_HE + 2 * s0) : (CV_HO + 2 * (s0 + 1));
    const int boff = CV_XB0 + 32 * (r - gh) + 16 * g0 - 448;
    const int eoff = CV_XB0 + 32 * r + 16 * gh + 8 * (1 - g0);
    u32x4 nx[4], nh[4];
    {
        const int ch = c0 + wid * 4;
#pragma unroll
        for (int i = 0; i < 4; ++i) { nx[i] = *(const u32x4*)(kvT + (size_t)ch * SEQ + (i * 64 + lane) * 8); nh[i] = *(const u32x4*)(ssmk + (size_t)ch * SEQ + (i * 64 + lane) * 8); }
    }
    for (int cc = 0; cc < 4; ++cc) {
        const int ch = c0 + wid * 4 + cc;
        {
            unsigned rot[4];
#pragma unroll
            for (int i = 0; i < 4; ++i) rot[i] = (unsigned)__builtin_amdgcn_update_dpp(0, (int)nh[i][3], 0x13C  , 0xf, 0xf, false);
#pragma unroll
            for (int i = 0; i < 4; ++i) {
                const unsigned prev = (lane == 0) ? (i == 0 ? 0u : rot[i > 0 ? i - 1 : 0]) : rot[i];
                u32x4 o;
                o[0] = __builtin_amdgcn_alignbit(nh[i][0], prev, 16);
                o[1] = __builtin_amdgcn_alignbit(nh[i][1], nh[i][0], 16);
                o[2] = __builtin_amdgcn_alignbit(nh[i][2], nh[i][1], 16);
                o[3] = __builtin_amdgcn_alignbit(nh[i][3], nh[i][2], 16);
                *(LAS u32x4*)(wl + CV_XB0 + (i * 64 + lane) * 16) = nx[i];
                *(LAS u32x4*)(wl + CV_HE + 32 + (i * 64 + lane) * 16) = nh[i];
                *(LAS u32x4*)(wl + CV_HO + 32 + (i * 64 + lane) * 16) = o;
            }
        }
        const float dcoef = bf_lo((unsigned)ssmD[ch]);
        if (cc < 3) {
#pragma unroll
            for (int i = 0; i < 4; ++i) { nx[i] = *(const u32x4*)(kvT + (size_t)(ch + 1) * SEQ + (i * 64 + lane) * 8); nh[i] = *(const u32x4*)(ssmk + (size_t)(ch + 1) * SEQ + (i * 64 + lane) * 8); }
        }
        f32x4 acc[8];
#pragma unroll
        for (int c = 0; c < 8; ++c) acc[c] = (f32x4){0.f, 0.f, 0.f, 0.f};
#pragma unroll
        for (int v = 0; v < 8; ++v) {
            bf16x8 af[8], bf[8];
#pragma unroll
            for (int u = 0; u < 8; ++u) {
                const LAS unsigned* pa = (const LAS unsigned*)(wl + aoff + 64 * (8 * u + v));
                u32x4 t; t[0] = pa[0]; t[1] = pa[1]; t[2] = pa[2]; t[3] = pa[3];
                af[u] = __builtin_bit_cast(bf16x8, t);
                bf[u] = *(const LAS bf16x8*)(wl + boff + 512 * u + 64 * (7 - v));
            }
#pragma unroll
            for (int c = 0; c < 8; ++c)
#pragma unroll
                for (int u = 0; u <= c; ++u) acc[c] = __builtin_amdgcn_mfma_f32_16x16x32_bf16(af[u], bf[c - u], acc[c], 0, 0, 0);
        }
#pragma unroll
        for (int c = 0; c < 8; ++c) {
            const u32x2 z = *(const LAS u32x2*)(wl + eoff + 512 * c);
            u32x2 o;
            o[0] = pack_bf16(acc[c][0] + bf_hi(z[1]) * dcoef, acc[c][1] + bf_lo(z[1]) * dcoef);
            o[1] = pack_bf16(acc[c][2] + bf_hi(z[0]) * dcoef, acc[c][3] + bf_lo(z[0]) * dcoef);
            *(u32x2*)(y2T + (size_t)ch * SEQ + 256 * c + 16 * r + 4 * g) = o;
        }
    }
}

constexpr int HTB = 16384;
__global__ __launch_bounds__(512, 2) void k2_gemm1(const u16* __restrict__ xn, const u16* __restrict__ wg, const u16* __restrict__ wu, u16* __restrict__ hbuf,
                                                   const u16* __restrict__ kin, const u16* __restrict__ vin, const u16* __restrict__ shk, const u16* __restrict__ ssmk,
                                                   const u16* __restrict__ ssmD, u16* __restrict__ kvT, u16* __restrict__ y2T) {
    extern __shared__ __attribute__((aligned(16))) unsigned char shm[];
    LAS unsigned char* lds = (LAS unsigned char*)shm;
    const int xcd = blockIdx.x & 7, bi = blockIdx.x >> 3;
    if (bi >= 28) { conv_block(lds, xcd * 4 + (bi - 28), kin, vin, shk, ssmk, ssmD, kvT, y2T); return; }
    const int pm = 4 * (xcd & 1) + (bi & 3), pn = 7 * (xcd >> 1) + (bi >> 2);
    const int tid = threadIdx.x, wid = __builtin_amdgcn_readfirstlane(tid >> 6), lane = tid & 63, wr = wid >> 2, wc = wid & 3, fr = lane & 15, fq = lane >> 4;
    constexpr int K = DM, nt = K / 64;
    const unsigned voffA = (unsigned)(((tid >> 3) * K + 8 * ((tid & 7) ^ ((tid >> 3) & 7))) * 2);
    const int kb = tid >> 4;
    const unsigned voffB = (unsigned)((kb * FF + 8 * ((tid & 15) ^ (((kb & 3) << 2) | (((kb >> 3) & 1) << 1)))) * 2);
    const char* cA = (const char*)xn + (size_t)pm * 256 * K * 2;
    const char* cG = (const char*)wg + (size_t)pn * 128 * 2;
    const char* cU = (const char*)wu + (size_t)pn * 128 * 2;
    constexpr size_t AH = (size_t)128 * K * 2, AI = (size_t)64 * K * 2, AK = 128, BI = (size_t)32 * FF * 2, BK_ = (size_t)64 * FF * 2;
    const unsigned ldsw = (unsigned)wid * 1024u;
    const int q = (lane & 15) >> 2, p = lane & 3;
    const int aoff0 = (wr * 64 + fr) * 128 + 16 * ((fq) ^ (fr & 7)), aoff1 = (wr * 64 + fr) * 128 + 16 * ((4 + fq) ^ (fr & 7));
    const int boff0 = 256 * (8 * fq + q) + 16 * (4 * (wc ^ q) + 2 * (0 ^ (fq & 1)) + (p >> 1)) + 8 * (p & 1);
    const int boff1 = 256 * (8 * fq + q) + 16 * (4 * (wc ^ q) + 2 * (1 ^ (fq & 1)) + (p >> 1)) + 8 * (p & 1);
#define SA(b, h) (((b) * 2 + (h)) * HTB)
#define SB(b, h) ((4 + (b) * 2 + (h)) * HTB)
#define STAGE_A(bufoff, h, kt) do { const char* _g = cA + (h) * AH + (size_t)(kt) * AK; \
        GLDS(_g + voffA, lds + (bufoff) + ldsw); GLDS(_g + AI + voffA, lds + (bufoff) + ldsw + 8192); } while (0)
#define STAGE_B(bufoff, base, kt) do { const char* _g = (base) + (size_t)(kt) * BK_; \
        GLDS(_g + voffB, lds + (bufoff) + ldsw); GLDS(_g + BI + voffB, lds + (bufoff) + ldsw + 8192); } while (0)
#define LDA(dst, b, h) do { _Pragma("unroll") for (int m = 0; m < 4; ++m) { \
        dst[m][0] = *(const LAS bf16x8*)(lds + SA(b, h) + aoff0 + m * 2048); dst[m][1] = *(const LAS bf16x8*)(lds + SA(b, h) + aoff1 + m * 2048); } } while (0)
#define LDB(dst, b, h) do { \
        TRRD(dst[0][0][0], bb0, SB(b, h) - SB(0, 0)); TRRD(dst[0][0][1], bb0, SB(b, h) - SB(0, 0) + 1024); TRRD(dst[1][0][0], bb1, SB(b, h) - SB(0, 0)); TRRD(dst[1][0][1], bb1, SB(b, h) - SB(0, 0) + 1024); \
        TRRD(dst[0][1][0], bb0, SB(b, h) - SB(0, 0) + 8192); TRRD(dst[0][1][1], bb0, SB(b, h) - SB(0, 0) + 9216); TRRD(dst[1][1][0], bb1, SB(b, h) - SB(0, 0) + 8192); TRRD(dst[1][1][1], bb1, SB(b, h) - SB(0, 0) + 9216); } while (0)
#define WAIT_LB(dst) asm volatile("s_waitcnt lgkmcnt(0)" : "+v"(dst[0][0][0]), "+v"(dst[0][0][1]), "+v"(dst[0][1][0]), "+v"(dst[0][1][1]), "+v"(dst[1][0][0]), "+v"(dst[1][0][1]), "+v"(dst[1][1][0]), "+v"(dst[1][1][1]) :: "memory")
#define MMA(ai, bj, At, Bt) do { __builtin_amdgcn_s_setprio(1); _Pragma("unroll") for (int m = 0; m < 4; ++m) _Pragma("unroll") for (int n = 0; n < 2; ++n) _Pragma("unroll") for (int k = 0; k < 2; ++k) \
        acc[ai][bj][m][n] = __builtin_amdgcn_mfma_f32_16x16x32_bf16(CAT(Bt[n][k][0], Bt[n][k][1]), At[m][k], acc[ai][bj][m][n], 0, 0, 0); __builtin_amdgcn_s_setprio(0); } while (0)
    f32x4 acc[2][2][4][2];
#pragma unroll
    for (int a = 0; a < 2; ++a)
#pragma unroll
        for (int b = 0; b < 2; ++b)
#pragma unroll
            for (int m = 0; m < 4; ++m)
#pragma unroll
                for (int n = 0; n < 2; ++n) acc[a][b][m][n] = (f32x4){0.f, 0.f, 0.f, 0.f};
    bf16x8 At[4][2]; bf16x4 B0[2][2][2], B1[2][2][2];
    const unsigned bb0 = (unsigned)(uintptr_t)(lds + SB(0, 0) + boff0), bb1 = (unsigned)(uintptr_t)(lds + SB(0, 0) + boff1);
    STAGE_B(SB(0, 0), cG, 0); STAGE_A(SA(0, 0), 0, 0); STAGE_B(SB(0, 1), cU, 0); STAGE_A(SA(0, 1), 1, 0);
    if (wr == 1) BAR();
    WAIT_V(4); BAR();
    STAGE_B(SB(1, 0), cG, 1); STAGE_A(SA(1, 0), 0, 1); STAGE_B(SB(1, 1), cU, 1);
    WAIT_V(6); BAR();
    for (int t = 0; t < nt - 2; t += 2) {
        LDB(B0, 0, 0); SCHED(); LDA(At, 0, 0); STAGE_A(SA(1, 1), 1, t + 1);
        WAIT_L(8); BAR(); WAIT_LB(B0); SCHED(); MMA(0, 0, At, B0); BAR(); SCHED();
        LDB(B1, 0, 1); STAGE_B(SB(0, 0), cG, t + 2);
        BAR(); WAIT_LB(B1); SCHED(); MMA(0, 1, At, B1); BAR();
        LDA(At, 0, 1); STAGE_A(SA(0, 0), 0, t + 2);
        BAR(); WAIT_L(0); MMA(1, 0, At, B0); BAR(); SCHED();
        STAGE_B(SB(0, 1), cU, t + 2);
        WAIT_V(6); BAR(); MMA(1, 1, At, B1); BAR();
        LDB(B0, 1, 0); SCHED(); LDA(At, 1, 0); STAGE_A(SA(0, 1), 1, t + 2);
        WAIT_L(8); BAR(); WAIT_LB(B0); SCHED(); MMA(0, 0, At, B0); BAR(); SCHED();
        LDB(B1, 1, 1); STAGE_B(SB(1, 0), cG, t + 3);
        BAR(); WAIT_LB(B1); SCHED(); MMA(0, 1, At, B1); BAR();
        LDA(At, 1, 1); STAGE_A(SA(1, 0), 0, t + 3);
        BAR(); WAIT_L(0); MMA(1, 0, At, B0); BAR(); SCHED();
        STAGE_B(SB(1, 1), cU, t + 3);
        WAIT_V(6); BAR(); MMA(1, 1, At, B1); BAR();
    }
    {
        LDB(B0, 0, 0); SCHED(); LDA(At, 0, 0); STAGE_A(SA(1, 1), 1, nt - 1);
        WAIT_L(8); BAR(); WAIT_LB(B0); SCHED(); MMA(0, 0, At, B0); BAR(); SCHED();
        LDB(B1, 0, 1); BAR(); WAIT_LB(B1); SCHED(); MMA(0, 1, At, B1); BAR();
        LDA(At, 0, 1); BAR(); WAIT_L(0); MMA(1, 0, At, B0); BAR(); SCHED();
        WAIT_V(0); BAR(); MMA(1, 1, At, B1); BAR();
    }
    {
        LDB(B0, 1, 0); SCHED(); LDA(At, 1, 0); BAR(); WAIT_LB(B0); SCHED(); MMA(0, 0, At, B0); BAR(); SCHED();
        LDB(B1, 1, 1); BAR(); WAIT_LB(B1); SCHED(); MMA(0, 1, At, B1); BAR();
        LDA(At, 1, 1); BAR(); WAIT_L(0); MMA(1, 0, At, B0); MMA(1, 1, At, B1);
    }
    if (wr == 0) BAR();
    int tz = threadIdx.x; asm volatile("" : "+v"(tz));
    const int el = tz & 63, efr = el & 15, efq = el >> 4;
    u16* hrow = hbuf + (size_t)(pm * 256 + wr * 64 + efr) * FF + pn * 128 + wc * 32 + 16 * (efq & 1) + 8 * (efq >> 1);
#pragma unroll
    for (int ai = 0; ai < 2; ++ai)
#pragma unroll
        for (int m = 0; m < 4; ++m) {
            unsigned pk[2][2];
#pragma unroll
            for (int n = 0; n < 2; ++n) {
                float hv[4];
#pragma unroll
                for (int j = 0; j < 4; ++j) {
                    const float e = __builtin_amdgcn_exp2f(acc[ai][0][m][n][j] * -1.4426950408889634f);
                    hv[j] = acc[ai][1][m][n][j] * __builtin_amdgcn_rcpf(1.0f + e);
                }
                pk[n][0] = pack_bf16(hv[0], hv[1]); pk[n][1] = pack_bf16(hv[2], hv[3]);
            }
            const auto s0 = __builtin_amdgcn_permlane16_swap(pk[0][0], pk[1][0], false, false);
            const auto s1 = __builtin_amdgcn_permlane16_swap(pk[0][1], pk[1][1], false, false);
            *(u32x4*)(hrow + (size_t)(ai * 128 + m * 16) * FF) = (u32x4){s0[0], s1[0], s0[1], s1[1]};
        }
#undef SA
#undef SB
#undef STAGE_A
#undef STAGE_B
#undef LDA
#undef LDB
#undef WAIT_LB
#undef MMA
}

constexpr int K3_NS = 6, K3_STAGE = 24576, K3_Y = K3_NS * K3_STAGE, K3_LDS = K3_Y + 16384;
__global__ __launch_bounds__(512, 2) void k3_gemm2(const u16* __restrict__ hbuf, const u16* __restrict__ wd, const u16* __restrict__ x, const f32x2* __restrict__ stats,
                                                   const u16* __restrict__ y2T, u16* __restrict__ out) {
    extern __shared__ __attribute__((aligned(16))) unsigned char shm[];
    LAS unsigned char* lds = (LAS unsigned char*)shm;
    const int xcd = blockIdx.x & 7, bi = blockIdx.x >> 3;
    const int pm = 4 * (xcd & 3) + (bi & 3), pn = 8 * (xcd >> 2) + (bi >> 2);
    const int tid = threadIdx.x, wid = __builtin_amdgcn_readfirstlane(tid >> 6), lane = tid & 63;
    const int kg = wid >> 2, wr = (wid >> 1) & 1, wc = wid & 1, fr = lane & 15, fq = lane >> 4, q = (lane & 15) >> 2, p = lane & 3;
    constexpr int NT = FF / 64;
    const int ecol = pn * 64 + wc * 32 + 16 * (fq & 1) + 8 * (fq >> 1);
    const int erow = pm * 128 + wr * 64 + kg * 32 + fr;
    u32x4 xv[2]; f32x2 st[2];
#pragma unroll
    for (int m = 0; m < 2; ++m) { xv[m] = *(const u32x4*)(x + (size_t)(erow + 16 * m) * DM + ecol); st[m] = stats[erow + 16 * m]; }
    {
#pragma unroll
        for (int i = 0; i < 2; ++i) {
            const int row = (wid * 2 + i) * 4 + (lane >> 4), ck = (lane & 15) ^ ((row & 7) << 1);
            GLDS((const char*)y2T + ((size_t)(pn * 64 + row) * SEQ + pm * 128 + 8 * ck) * 2, lds + K3_Y + (wid * 2 + i) * 1024);
        }
    }
    const unsigned voffA = (unsigned)(((wid * 16 + (lane >> 3)) * FF + 8 * ((lane & 7) ^ ((lane >> 3) & 7))) * 2);
    const int krow = wid * 8 + (lane >> 3);
    const unsigned voffB = (unsigned)((krow * DM + 8 * ((lane & 7) ^ ((((krow >> 1) & 1) << 2) | (((krow >> 3) & 1) << 1)))) * 2);
    const char* cA = (const char*)hbuf + (size_t)pm * 128 * FF * 2;
    const char* cB = (const char*)wd + (size_t)pn * 64 * 2;
#define K3_ISSUE(t, slot) do { const char* _a = cA + (size_t)(t) * 128; const char* _b = cB + (size_t)(t) * 64 * DM * 2; LAS unsigned char* _l = lds + (slot) * K3_STAGE; \
        GLDS(_a + voffA, _l + wid * 2048); GLDS(_a + (size_t)8 * FF * 2 + voffA, _l + wid * 2048 + 1024); GLDS(_b + voffB, _l + 16384 + wid * 1024); } while (0)
    const int aoff = (wr * 64 + fr) * 128 + 16 * ((4 * kg + fq) ^ (fr & 7));
    const int brow = 32 * kg + 8 * fq + q;
    const int boff0 = 16384 + 128 * brow + 16 * (4 * (wc ^ (q >> 1)) + 2 * (0 ^ (fq & 1)) + (p >> 1)) + 8 * (p & 1);
    const int boff1 = 16384 + 128 * brow + 16 * (4 * (wc ^ (q >> 1)) + 2 * (1 ^ (fq & 1)) + (p >> 1)) + 8 * (p & 1);
    const unsigned ldsb = (unsigned)(uintptr_t)lds;
#define K3_READ(Af, Bf, slot) do { const LAS unsigned char* _l = lds + (slot) * K3_STAGE; const unsigned _b0 = ldsb + (slot) * K3_STAGE + boff0, _b1 = ldsb + (slot) * K3_STAGE + boff1; \
        TRRD(Bf[0][0], _b0, 0); TRRD(Bf[0][1], _b0, 512); TRRD(Bf[1][0], _b1, 0); TRRD(Bf[1][1], _b1, 512); \
        _Pragma("unroll") for (int m = 0; m < 4; ++m) Af[m] = *(const LAS bf16x8*)(_l + aoff + m * 2048); } while (0)
#define K3_WAITL(Bf) asm volatile("s_waitcnt lgkmcnt(0)" : "+v"(Bf[0][0]), "+v"(Bf[0][1]), "+v"(Bf[1][0]), "+v"(Bf[1][1]) :: "memory")
#define K3_MMA(Af, Bf) do { _Pragma("unroll") for (int m = 0; m < 4; ++m) _Pragma("unroll") for (int n = 0; n < 2; ++n) \
        acc[m][n] = __builtin_amdgcn_mfma_f32_16x16x32_bf16(CAT(Bf[n][0], Bf[n][1]), Af[m], acc[m][n], 0, 0, 0); } while (0)
    f32x4 acc[4][2];
#pragma unroll
    for (int m = 0; m < 4; ++m) { acc[m][0] = (f32x4){0.f, 0.f, 0.f, 0.f}; acc[m][1] = (f32x4){0.f, 0.f, 0.f, 0.f}; }
    bf16x8 A0[4], A1[4]; bf16x4 B0[2][2], B1[2][2];
#pragma unroll
    for (int s = 0; s < K3_NS - 1; ++s) K3_ISSUE(s, s);
    WAIT_V(12); BAR();
    K3_READ(A0, B0, 0);
    int sc = 0;
#define K3_STEP(t, VM, ISS, Ac, Bc, An, Bn) do { \
        const int _sn = (sc + 1 == K3_NS) ? 0 : sc + 1, _sp = (sc == 0) ? K3_NS - 1 : sc - 1; \
        WAIT_V(VM); K3_WAITL(Bc); BAR(); \
        if (ISS) K3_ISSUE((t) + K3_NS - 1, _sp); \
        K3_READ(An, Bn, _sn); SCHED(); \
        K3_MMA(Ac, Bc); SCHED(); \
        sc = _sn; } while (0)
    for (int t = 0; t < NT - 6; t += 2) {
        K3_STEP(t, 9, true, A0, B0, A1, B1);
        K3_STEP(t + 1, 9, true, A1, B1, A0, B0);
    }
    K3_STEP(50, 9, true, A0, B0, A1, B1);
    K3_STEP(51, 9, false, A1, B1, A0, B0);
    K3_STEP(52, 6, false, A0, B0, A1, B1);
    K3_STEP(53, 3, false, A1, B1, A0, B0);
    K3_STEP(54, 0, false, A0, B0, A1, B1);
    K3_WAITL(B1); SCHED(); K3_MMA(A1, B1);
    BAR();
    {
        LAS f32x4* ex = (LAS f32x4*)(lds + wid * 4096);
        const LAS f32x4* in = (const LAS f32x4*)(lds + (wid ^ 4) * 4096);
        f32x4 keep[2][2], give[2][2];
#pragma unroll
        for (int m = 0; m < 2; ++m)
#pragma unroll
            for (int n = 0; n < 2; ++n) { keep[m][n] = kg ? acc[2 + m][n] : acc[m][n]; give[m][n] = kg ? acc[m][n] : acc[2 + m][n]; }
#pragma unroll
        for (int m = 0; m < 2; ++m)
#pragma unroll
            for (int n = 0; n < 2; ++n) ex[(m * 2 + n) * 64 + lane] = give[m][n];
        WAIT_L(0); BAR();
#pragma unroll
        for (int m = 0; m < 2; ++m) {
            float v[8];
            const f32x4 a0 = keep[m][0] + in[(m * 2 + 0) * 64 + lane], a1 = keep[m][1] + in[(m * 2 + 1) * 64 + lane];
#pragma unroll
            for (int j = 0; j < 4; ++j) {
                const auto s = __builtin_amdgcn_permlane16_swap(__float_as_uint(a0[j]), __float_as_uint(a1[j]), false, false);
                v[j] = __uint_as_float(s[0]); v[4 + j] = __uint_as_float(s[1]);
            }
            const int crow = wc * 32 + 16 * (fq & 1) + 8 * (fq >> 1) + q;
            const int tck = (wr * 64 + kg * 32 + 16 * m) / 8 + (p >> 1);
            const LAS unsigned char* yb = lds + K3_Y;
            const bf16x4 y0 = __builtin_amdgcn_ds_read_tr16_b64_v4i16((LAS bf16x4*)(yb + crow * 256 + 16 * (tck ^ ((crow & 7) << 1)) + 8 * (p & 1)));
            const bf16x4 y1 = __builtin_amdgcn_ds_read_tr16_b64_v4i16((LAS bf16x4*)(yb + (crow + 4) * 256 + 16 * (tck ^ (((crow + 4) & 7) << 1)) + 8 * (p & 1)));
            const float mb = st[m][0], rs = st[m][1];
            float o[8];
#pragma unroll
            for (int j = 0; j < 8; ++j) {
                const unsigned xw = xv[m][j >> 1];
                const float xf = (j & 1) ? bf_hi(xw) : bf_lo(xw);
                const float xn = bf_round(bf_round(xf - mb) * rs);
                const float yf = __uint_as_float((unsigned)(u16)(j < 4 ? y0[j & 3] : y1[j & 3]) << 16);
                o[j] = bf_round(xf + bf_round(xn * yf)) + bf_round(v[j]);
            }
            *(u32x4*)(out + (size_t)(erow + 16 * m) * DM + ecol) = (u32x4){pack_bf16(o[0], o[1]), pack_bf16(o[2], o[3]), pack_bf16(o[4], o[5]), pack_bf16(o[6], o[7])};
        }
    }
#undef K3_ISSUE
#undef K3_READ
#undef K3_MMA
#undef K3_WAITL
#undef K3_STEP
}

extern "C" void kernel_launch(void* const* d_in, const int* in_sizes, int n_in, void* d_out, int out_size, void* d_ws, size_t ws_size, hipStream_t stream) {
    const u16* q = (const u16*)d_in[0];
    const u16* k = (const u16*)d_in[1];
    const u16* v = (const u16*)d_in[2];
    const u16* shk = (const u16*)d_in[3];
    const u16* ssmk = (const u16*)d_in[4];
    const u16* ssmD = (const u16*)d_in[5];
    const u16* wg = (const u16*)d_in[7];
    const u16* wu = (const u16*)d_in[8];
    const u16* wd = (const u16*)d_in[9];
    unsigned char* ws = (unsigned char*)d_ws;
    const size_t MB = 1u << 20;
    u16* xn = (u16*)(ws + 0 * MB);
    u16* kvT = (u16*)(ws + 4 * MB);
    u16* y2T = (u16*)(ws + 8 * MB);
    f32x2* stats = (f32x2*)(ws + 12 * MB);
    u16* hbuf = (u16*)(ws + 16 * MB);
    (void)hipFuncSetAttribute((const void*)k2_gemm1, hipFuncAttributeMaxDynamicSharedMemorySize, 131072);
    (void)hipFuncSetAttribute((const void*)k3_gemm2, hipFuncAttributeMaxDynamicSharedMemorySize, K3_LDS);
    k1_ln<<<SEQ / 4, 256, 0, stream>>>(q, xn, stats);
    k2_gemm1<<<256, 512, 131072, stream>>>(xn, wg, wu, hbuf, k, v, shk, ssmk, ssmD, kvT, y2T);
    k3_gemm2<<<256, 512, K3_LDS, stream>>>(hbuf, wd, q, stats, y2T, (u16*)d_out);
}
```

```cpp
#include <hip/hip_runtime.h>
#include <hip/hip_bf16.h>
#include <stdint.h>

#define LAS __attribute__((address_space(3)))
#define GAS __attribute__((address_space(1)))
typedef unsigned short u16;
typedef short bf16x8 __attribute__((ext_vector_type(8)));
typedef short bf16x4 __attribute__((ext_vector_type(4)));
typedef float f32x4 __attribute__((ext_vector_type(4)));
typedef float f32x2 __attribute__((ext_vector_type(2)));
typedef unsigned u32x4 __attribute__((ext_vector_type(4)));
typedef unsigned u32x2 __attribute__((ext_vector_type(2)));
typedef __bf16 bf16x2_t __attribute__((ext_vector_type(2)));
typedef unsigned long long u64;
typedef GAS u64 gu64;

constexpr int SEQ = 2048, DM = 1024, FF = 3584;

__device__ __forceinline__ unsigned pack_bf16(float lo, float hi) {
    f32x2 v = {lo, hi};
    return __builtin_bit_cast(unsigned, __builtin_convertvector(v, bf16x2_t));
}
__device__ __forceinline__ float bf_lo(unsigned u) { return __uint_as_float(u << 16); }
__device__ __forceinline__ float bf_hi(unsigned u) { return __uint_as_float(u & 0xffff0000u); }
__device__ __forceinline__ float bf_round(float f) { return bf_lo(pack_bf16(f, f)); }

typedef __amdgpu_buffer_rsrc_t rsrc_t;
__device__ __forceinline__ rsrc_t mk_rsrc(const void* base, unsigned bytes) { return __builtin_amdgcn_make_buffer_rsrc((void*)base, 0, bytes, 0x00020000); }
__device__ __forceinline__ void st16(rsrc_t r, unsigned off, u32x4 v) { __builtin_amdgcn_raw_buffer_store_b128(v, r, off, 0, 16); }
__device__ __forceinline__ void st8(rsrc_t r, unsigned off, u32x2 v) { __builtin_amdgcn_raw_buffer_store_b64(v, r, off, 0, 16); }
__device__ __forceinline__ u32x4 ld16_stream(const void* p) { return __builtin_nontemporal_load((const u32x4*)p); }

#define WAIT_V(n) asm volatile("s_waitcnt vmcnt(" #n ")" ::: "memory")
#define WAIT_L(n) asm volatile("s_waitcnt lgkmcnt(" #n ")" ::: "memory")
#define BAR() __builtin_amdgcn_s_barrier()
#define SCHED() __builtin_amdgcn_sched_barrier(0)
#define GLDS(gptr, ldsptr) __builtin_amdgcn_global_load_lds((const GAS unsigned*)(gptr), (LAS unsigned*)(ldsptr), 16, 0, 0)
#define TRRD(dst, addr, off) asm volatile("ds_read_b64_tr_b16 %0, %1 offset:%c2" : "=&v"(dst) : "v"(addr), "i"(off) : "memory")
#define RD128(dst, addr, off) asm volatile("ds_read_b128 %0, %1 offset:%c2" : "=&v"(dst) : "v"(addr), "i"(off) : "memory")
#define CAT(lo, hi) __builtin_shufflevector(lo, hi, 0, 1, 2, 3, 4, 5, 6, 7)

#define ST()

struct Params {
    const u16 *x, *kin, *vin, *shk, *ssmk, *ssmD, *wg, *wu, *wd;
    u16 *out, *xn, *kvT, *y2T, *hbuf;
    f32x2* stats;
    float* part;
    u64* sync;
    u64* stamps;
};
constexpr int SY_EPOCH = 0, SY_X = 128, SY_K = 512, SY_H = 1024, SY_Y = 1536, SY_P = 2048;

__device__ __forceinline__ void signal(u64* word, u64 tag) { __hip_atomic_store((gu64*)word, tag, __ATOMIC_RELAXED, __HIP_MEMORY_SCOPE_AGENT); }
__device__ __forceinline__ void poll_words(const u64* word, bool want, u64 tag) {
    const u64 t0 = __builtin_amdgcn_s_memrealtime();
    for (;;) {
        const u64 v = want ? __hip_atomic_load((gu64*)word, __ATOMIC_RELAXED, __HIP_MEMORY_SCOPE_AGENT) : tag;
        if (__all(v == tag)) break;
        if (__builtin_amdgcn_s_memrealtime() - t0 > 5000000ull) break;
        __builtin_amdgcn_s_sleep(2);
    }
}
#define ACQUIRE() do { __builtin_amdgcn_fence(__ATOMIC_ACQUIRE, "agent"); WAIT_V(0); } while (0)

constexpr int CV_XB = 0, CV_XB0 = 512, CV_HE = 4608, CV_HO = 4608 + 4160, CV_WAVE = 4608 + 2 * 4160;
__device__ __forceinline__ void conv_block(const Params& P, LAS unsigned char* lds, int cb, int& _sti) {
    const int tid = threadIdx.x, c0 = cb * 32;
    const int wid = __builtin_amdgcn_readfirstlane(tid >> 6), lane = tid & 63;
    LAS unsigned char* wl = lds + wid * CV_WAVE;
    if (lane < 32) *(LAS u32x4*)(wl + CV_XB + lane * 16) = (u32x4){0u, 0u, 0u, 0u};
    if (lane < 2) { *(LAS u32x4*)(wl + CV_HE + lane * 16) = (u32x4){0u, 0u, 0u, 0u}; *(LAS u32x4*)(wl + CV_HO + lane * 16) = (u32x4){0u, 0u, 0u, 0u}; }
    const int r = lane & 15, g = lane >> 4, g0 = g & 1, gh = g >> 1;
    const int s0 = r - 8 * g0 + 9 + 16 * gh;
    const int aoff = (r & 1) ? (CV_HE + 2 * s0) : (CV_HO + 2 * (s0 + 1));
    const int boff = CV_XB0 + 32 * (r - gh) + 16 * g0 - 448;
    const rsrc_t ry = mk_rsrc(P.y2T, SEQ * DM * 2);
    LAS unsigned char* abase[4];
#pragma unroll
    for (int i = 0; i < 4; ++i) { unsigned a = (unsigned)(uintptr_t)(wl + aoff + 1024 * i); asm volatile("" : "+v"(a)); abase[i] = (LAS unsigned char*)(uintptr_t)a; }
    unsigned dmask[4];
#pragma unroll
    for (int i = 0; i < 4; ++i) dmask[i] = (gh == 0 && 8 * g0 + 7 - 2 * i == r) ? 0x0000ffffu : (gh == 0 && 8 * g0 + 6 - 2 * i == r) ? 0xffff0000u : 0u;
    const int chw = c0 + wid * 4;
    const u32x2 dpair = *(const u32x2*)(P.ssmD + chw);
    const u16* kvT = P.kvT; const u16* ssmk = P.ssmk;
    u32x4 nx[4], nh[4];
#define CV_GLOAD(ch) do { _Pragma("unroll") for (int i = 0; i < 4; ++i) { nx[i] = *(const u32x4*)(kvT + (size_t)(ch) * SEQ + (i * 64 + lane) * 8); nh[i] = ld16_stream(ssmk + (size_t)(ch) * SEQ + (i * 64 + lane) * 8); } } while (0)
#define CV_STAGE() do { unsigned rot[4]; \
        _Pragma("unroll") for (int i = 0; i < 4; ++i) rot[i] = (unsigned)__builtin_amdgcn_update_dpp(0, (int)nh[i][3], 0x13C  , 0xf, 0xf, false); \
        _Pragma("unroll") for (int i = 0; i < 4; ++i) { \
            const unsigned prev = (lane == 0) ? (i == 0 ? 0u : rot[i > 0 ? i - 1 : 0]) : rot[i]; \
            u32x4 o; \
            o[0] = __builtin_amdgcn_alignbit(nh[i][0], prev, 16); o[1] = __builtin_amdgcn_alignbit(nh[i][1], nh[i][0], 16); \
            o[2] = __builtin_amdgcn_alignbit(nh[i][2], nh[i][1], 16); o[3] = __builtin_amdgcn_alignbit(nh[i][3], nh[i][2], 16); \
            *(LAS u32x4*)(wl + CV_XB0 + (i * 64 + lane) * 16) = nx[i]; \
            *(LAS u32x4*)(wl + CV_HE + 32 + (i * 64 + lane) * 16) = nh[i]; \
            *(LAS u32x4*)(wl + CV_HO + 32 + (i * 64 + lane) * 16) = o; } } while (0)
#define CV_LOAD(buf, v) do { _Pragma("unroll") for (int u = 0; u < 8; ++u) { \
            bf[buf][u] = *(const LAS bf16x8*)(wl + boff + 512 * u + 64 * (7 - (v))); \
            const LAS unsigned* pa = (const LAS unsigned*)(abase[u >> 1] + 64 * (8 * (u & 1) + (v))); \
            u32x4 t; t[0] = pa[0]; t[1] = pa[1]; t[2] = pa[2]; t[3] = pa[3]; \
            af[buf][u] = __builtin_bit_cast(bf16x8, t); } } while (0)
#define CV_EPI(a, ch, c) do { u32x2 o; o[0] = pack_bf16(acc[a][c][0], acc[a][c][1]); o[1] = pack_bf16(acc[a][c][2], acc[a][c][3]); \
            st8(ry, (unsigned)(((ch) * SEQ + 256 * (c) + 16 * r + 4 * g) * 2), o); } while (0)
    bf16x8 af[2][8], bf[2][8];
    f32x4 acc[2][8];
    CV_GLOAD(chw);
    CV_STAGE();
    CV_LOAD(0, 0);
    CV_GLOAD(chw + 1);
    SCHED();
#pragma unroll
    for (int cc = 0; cc < 4; ++cc) {
        const int a = cc & 1;
        const unsigned dw = dpair[cc >> 1], dd = (cc & 1) ? ((dw & 0xffff0000u) | (dw >> 16)) : ((dw << 16) | (dw & 0xffffu));
        const u32x4 adw = {dd & dmask[0], dd & dmask[1], dd & dmask[2], dd & dmask[3]};
        const bf16x8 ad = __builtin_bit_cast(bf16x8, adw);
#pragma unroll
        for (int v = 0; v < 8; ++v) {
            const int cur = v & 1, nxt = cur ^ 1;
            if (v < 7) CV_LOAD(nxt, v + 1);
            else if (cc < 3) { CV_STAGE(); CV_LOAD(nxt, 0); if (cc < 2) CV_GLOAD(chw + cc + 2); }
            if (v == 0) {
#pragma unroll
                for (int c = 0; c < 8; ++c) acc[a][c] = __builtin_amdgcn_mfma_f32_16x16x32_bf16(ad, bf[cur][c], (f32x4){0.f, 0.f, 0.f, 0.f}, 0, 0, 0);
            }
#pragma unroll
            for (int u = 0; u < 8; ++u)
#pragma unroll
                for (int c = u; c < 8; ++c) acc[a][c] = __builtin_amdgcn_mfma_f32_16x16x32_bf16(af[cur][u], bf[cur][c - u], acc[a][c], 0, 0, 0);
            if (cc > 0 && v < 2) {
#pragma unroll
                for (int c = 0; c < 4; ++c) CV_EPI(a ^ 1, chw + cc - 1, 4 * v + c);
            }
            if (v == 7 && cc < 3) {
#pragma unroll
                for (int i = 0; i < 12; ++i) { __builtin_amdgcn_sched_group_barrier(0x008, 1, 0); __builtin_amdgcn_sched_group_barrier(0x002, 2, 0); __builtin_amdgcn_sched_group_barrier(0x200, 1, 0); }
#pragma unroll
                for (int i = 0; i < 24; ++i) { __builtin_amdgcn_sched_group_barrier(0x008, 1, 0); __builtin_amdgcn_sched_group_barrier(0x100, 1, 0); }
                __builtin_amdgcn_sched_group_barrier(0x020, 8, 0);
            } else if (v < 7) {
#pragma unroll
                for (int i = 0; i < 24; ++i) { __builtin_amdgcn_sched_group_barrier(0x008, 1, 0); __builtin_amdgcn_sched_group_barrier(0x100, 1, 0); }
                if (cc > 0 && v < 2) {
#pragma unroll
                    for (int i = 0; i < 4; ++i) { __builtin_amdgcn_sched_group_barrier(0x008, 1, 0); __builtin_amdgcn_sched_group_barrier(0x002, 2, 0); __builtin_amdgcn_sched_group_barrier(0x008, 1, 0); __builtin_amdgcn_sched_group_barrier(0x040, 1, 0); }
                }
            }
            SCHED();
        }
    }
#pragma unroll
    for (int c = 0; c < 8; ++c) CV_EPI(1, chw + 3, c);
    ST();
#undef CV_GLOAD
#undef CV_STAGE
#undef CV_LOAD
#undef CV_EPI
}

constexpr int HTB = 16384;
#define SA(b, h) (((b) * 2 + (h)) * HTB)
#define SB(b, h) ((4 + (b) * 2 + (h)) * HTB)
struct G1Addr { unsigned voffA, voffB; const char *cA, *cG, *cU; unsigned ldsw; };
#define STAGE_A(bufoff, h, kt) do { const char* _g = ga.cA + (h) * ((size_t)128 * DM * 2) + (size_t)(kt) * 128; \
        GLDS(_g + ga.voffA, lds + (bufoff) + ga.ldsw); GLDS(_g + (size_t)64 * DM * 2 + ga.voffA, lds + (bufoff) + ga.ldsw + 8192); } while (0)
#define STAGE_B(bufoff, base, kt) do { const char* _g = (base) + (size_t)(kt) * ((size_t)64 * FF * 2); \
        GLDS(_g + ga.voffB, lds + (bufoff) + ga.ldsw); GLDS(_g + (size_t)32 * FF * 2 + ga.voffB, lds + (bufoff) + ga.ldsw + 8192); } while (0)
__device__ __forceinline__ G1Addr gemm1_addr(const Params& P, int pm, int pn) {
    const int tid = threadIdx.x, wid = __builtin_amdgcn_readfirstlane(tid >> 6);
    G1Addr ga;
    ga.voffA = (unsigned)(((tid >> 3) * DM + 8 * ((tid & 7) ^ ((tid >> 3) & 7))) * 2);
    const int kb = tid >> 4;
    ga.voffB = (unsigned)((kb * FF + 8 * ((tid & 15) ^ (((kb & 3) << 2) | (((kb >> 3) & 1) << 1)))) * 2);
    ga.cA = (const char*)P.xn + (size_t)pm * 256 * DM * 2;
    ga.cG = (const char*)P.wg + (size_t)pn * 128 * 2;
    ga.cU = (const char*)P.wu + (size_t)pn * 128 * 2;
    ga.ldsw = (unsigned)wid * 1024u;
    return ga;
}
__device__ __forceinline__ void gemm1_block(const Params& P, LAS unsigned char* lds, const G1Addr& ga, int pm, int pn, int& _sti) {
    const int tid = threadIdx.x, wid = __builtin_amdgcn_readfirstlane(tid >> 6), lane = tid & 63, wr = wid >> 2, wc = wid & 3, fr = lane & 15, fq = lane >> 4;
    constexpr int nt = DM / 64;
    const int q = (lane & 15) >> 2, p = lane & 3;
    const int aoff0 = (wr * 64 + fr) * 128 + 16 * ((fq) ^ (fr & 7)), aoff1 = (wr * 64 + fr) * 128 + 16 * ((4 + fq) ^ (fr & 7));
    const int boff0 = 256 * (8 * fq + q) + 16 * (4 * (wc ^ q) + 2 * (0 ^ (fq & 1)) + (p >> 1)) + 8 * (p & 1);
    const int boff1 = 256 * (8 * fq + q) + 16 * (4 * (wc ^ q) + 2 * (1 ^ (fq & 1)) + (p >> 1)) + 8 * (p & 1);
#define LDA(dst, b, h) do { _Pragma("unroll") for (int m = 0; m < 4; ++m) { \
        dst[m][0] = *(const LAS bf16x8*)(lds + SA(b, h) + aoff0 + m * 2048); dst[m][1] = *(const LAS bf16x8*)(lds + SA(b, h) + aoff1 + m * 2048); } } while (0)
#define LDB(dst, b, h) do { \
        TRRD(dst[0][0][0], bb0, SB(b, h) - SB(0, 0)); TRRD(dst[0][0][1], bb0, SB(b, h) - SB(0, 0) + 1024); TRRD(dst[1][0][0], bb1, SB(b, h) - SB(0, 0)); TRRD(dst[1][0][1], bb1, SB(b, h) - SB(0, 0) + 1024); \
        TRRD(dst[0][1][0], bb0, SB(b, h) - SB(0, 0) + 8192); TRRD(dst[0][1][1], bb0, SB(b, h) - SB(0, 0) + 9216); TRRD(dst[1][1][0], bb1, SB(b, h) - SB(0, 0) + 8192); TRRD(dst[1][1][1], bb1, SB(b, h) - SB(0, 0) + 9216); } while (0)
#define WAIT_LB(dst) asm volatile("s_waitcnt lgkmcnt(0)" : "+v"(dst[0][0][0]), "+v"(dst[0][0][1]), "+v"(dst[0][1][0]), "+v"(dst[0][1][1]), "+v"(dst[1][0][0]), "+v"(dst[1][0][1]), "+v"(dst[1][1][0]), "+v"(dst[1][1][1]) :: "memory")
#define MMA(ai, bj, At, Bt) do { __builtin_amdgcn_s_setprio(1); _Pragma("unroll") for (int m = 0; m < 4; ++m) _Pragma("unroll") for (int n = 0; n < 2; ++n) _Pragma("unroll") for (int k = 0; k < 2; ++k) \
        acc[ai][bj][m][n] = __builtin_amdgcn_mfma_f32_16x16x32_bf16(CAT(Bt[n][k][0], Bt[n][k][1]), At[m][k], acc[ai][bj][m][n], 0, 0, 0); __builtin_amdgcn_s_setprio(0); } while (0)
    f32x4 acc[2][2][4][2];
#pragma unroll
    for (int a = 0; a < 2; ++a)
#pragma unroll
        for (int b = 0; b < 2; ++b)
#pragma unroll
            for (int m = 0; m < 4; ++m)
#pragma unroll
                for (int n = 0; n < 2; ++n) acc[a][b][m][n] = (f32x4){0.f, 0.f, 0.f, 0.f};
    bf16x8 At[4][2]; bf16x4 B0[2][2][2], B1[2][2][2];
    const unsigned bb0 = (unsigned)(uintptr_t)(lds + SB(0, 0) + boff0), bb1 = (unsigned)(uintptr_t)(lds + SB(0, 0) + boff1);
    STAGE_A(SA(0, 0), 0, 0); STAGE_A(SA(0, 1), 1, 0); STAGE_A(SA(1, 0), 0, 1);
    if (wr == 1) BAR();
    WAIT_V(2); BAR(); BAR();
    ST();
    for (int t = 0; t < nt - 2; t += 2) {
        LDB(B0, 0, 0); SCHED(); LDA(At, 0, 0); STAGE_A(SA(1, 1), 1, t + 1);
        WAIT_L(8); BAR(); WAIT_LB(B0); SCHED(); MMA(0, 0, At, B0); BAR(); SCHED();
        LDB(B1, 0, 1); STAGE_B(SB(0, 0), ga.cG, t + 2);
        BAR(); WAIT_LB(B1); SCHED(); MMA(0, 1, At, B1); BAR();
        LDA(At, 0, 1); STAGE_A(SA(0, 0), 0, t + 2);
        BAR(); WAIT_L(0); MMA(1, 0, At, B0); BAR(); SCHED();
        STAGE_B(SB(0, 1), ga.cU, t + 2);
        WAIT_V(6); BAR(); MMA(1, 1, At, B1); BAR();
        LDB(B0, 1, 0); SCHED(); LDA(At, 1, 0); STAGE_A(SA(0, 1), 1, t + 2);
        WAIT_L(8); BAR(); WAIT_LB(B0); SCHED(); MMA(0, 0, At, B0); BAR(); SCHED();
        LDB(B1, 1, 1); STAGE_B(SB(1, 0), ga.cG, t + 3);
        BAR(); WAIT_LB(B1); SCHED(); MMA(0, 1, At, B1); BAR();
        LDA(At, 1, 1); STAGE_A(SA(1, 0), 0, t + 3);
        BAR(); WAIT_L(0); MMA(1, 0, At, B0); BAR(); SCHED();
        STAGE_B(SB(1, 1), ga.cU, t + 3);
        WAIT_V(6); BAR(); MMA(1, 1, At, B1); BAR();
    }
    {
        LDB(B0, 0, 0); SCHED(); LDA(At, 0, 0); STAGE_A(SA(1, 1), 1, nt - 1);
        WAIT_L(8); BAR(); WAIT_LB(B0); SCHED(); MMA(0, 0, At, B0); BAR(); SCHED();
        LDB(B1, 0, 1); BAR(); WAIT_LB(B1); SCHED(); MMA(0, 1, At, B1); BAR();
        LDA(At, 0, 1); BAR(); WAIT_L(0); MMA(1, 0, At, B0); BAR(); SCHED();
        WAIT_V(0); BAR(); MMA(1, 1, At, B1); BAR();
    }
    {
        LDB(B0, 1, 0); SCHED(); LDA(At, 1, 0); BAR(); WAIT_LB(B0); SCHED(); MMA(0, 0, At, B0); BAR(); SCHED();
        LDB(B1, 1, 1); BAR(); WAIT_LB(B1); SCHED(); MMA(0, 1, At, B1); BAR();
        LDA(At, 1, 1); BAR(); WAIT_L(0); MMA(1, 0, At, B0); MMA(1, 1, At, B1);
    }
    if (wr == 0) BAR();
    ST();
    int tz = threadIdx.x; asm volatile("" : "+v"(tz));
    const int el = tz & 63, efr = el & 15, efq = el >> 4;
    {
        const int gb = (blockIdx.x & 7) * 28 + (blockIdx.x >> 3);
        if (wid < 4) __builtin_amdgcn_global_load_lds((const GAS unsigned*)((const char*)P.wd + ((size_t)(gb * 4 + wid) * 64 + el) * 128), (LAS unsigned*)(lds + SB(0, 0) + wid * 256), 4, 0, 0);
    }
    const rsrc_t rh = mk_rsrc(P.hbuf, SEQ * FF * 2);
    const unsigned hoff = (unsigned)(((pm * 256 + wr * 64 + efr) * FF + pn * 128 + wc * 32 + 16 * (efq & 1) + 8 * (efq >> 1)) * 2);
#pragma unroll
    for (int ai = 0; ai < 2; ++ai)
#pragma unroll
        for (int m = 0; m < 4; ++m) {
            unsigned pk[2][2];
#pragma unroll
            for (int n = 0; n < 2; ++n) {
                float hv[4];
#pragma unroll
                for (int j = 0; j < 4; ++j) {
                    const float e = __builtin_amdgcn_exp2f(acc[ai][0][m][n][j] * -1.4426950408889634f);
                    hv[j] = acc[ai][1][m][n][j] * __builtin_amdgcn_rcpf(1.0f + e);
                }
                pk[n][0] = pack_bf16(hv[0], hv[1]); pk[n][1] = pack_bf16(hv[2], hv[3]);
            }
            const auto s0 = __builtin_amdgcn_permlane16_swap(pk[0][0], pk[1][0], false, false);
            const auto s1 = __builtin_amdgcn_permlane16_swap(pk[0][1], pk[1][1], false, false);
            st16(rh, hoff + (unsigned)((ai * 128 + m * 16) * FF * 2), (u32x4){s0[0], s1[0], s0[1], s1[1]});
        }
    ST();
#undef LDA
#undef LDB
#undef WAIT_LB
#undef MMA
}

constexpr int KB_STAGE = 32768, KC_N1 = 23, LDS_BYTES = 5 * KB_STAGE;
#define KE_S01(XA, XB, YA, YB, ya0, ya1, yb0, yb1, yb2, yb3) do { \
        TRRD(YB[0][0][0], yb0, 0); TRRD(YB[0][0][1], yb0, 1024); RD128(YA[0][0], ya0, 0); TRRD(YB[0][1][0], yb1, 0); TRRD(YB[0][1][1], yb1, 1024); RD128(YA[0][1], ya0, 2048); TRRD(YB[0][2][0], yb2, 0); TRRD(YB[0][2][1], yb2, 1024); RD128(YA[0][2], ya0, 4096); TRRD(YB[0][3][0], yb3, 0); TRRD(YB[0][3][1], yb3, 1024); RD128(YA[0][3], ya0, 6144); TRRD(YB[1][0][0], yb0, 8192); TRRD(YB[1][0][1], yb0, 9216); RD128(YA[1][0], ya1, 0); TRRD(YB[1][1][0], yb1, 8192); TRRD(YB[1][1][1], yb1, 9216); RD128(YA[1][1], ya1, 2048); TRRD(YB[1][2][0], yb2, 8192); TRRD(YB[1][2][1], yb2, 9216); RD128(YA[1][2], ya1, 4096); TRRD(YB[1][3][0], yb3, 8192); TRRD(YB[1][3][1], yb3, 9216); RD128(YA[1][3], ya1, 6144); SCHED(); } while (0)
#define KE_S11(XA, XB, YA, YB, ya0, ya1, yb0, yb1, yb2, yb3) do { \
        KE_M(XA, XB, 0, 0, 0); SCHED(); TRRD(YB[0][0][0], yb0, 0); SCHED(); \
        KE_M(XA, XB, 0, 0, 1); SCHED(); TRRD(YB[0][0][1], yb0, 1024); SCHED(); \
        KE_M(XA, XB, 0, 0, 2); SCHED(); RD128(YA[0][0], ya0, 0); SCHED(); \
        KE_M(XA, XB, 0, 0, 3); SCHED(); TRRD(YB[0][1][0], yb1, 0); SCHED(); \
        KE_M(XA, XB, 0, 1, 0); SCHED(); TRRD(YB[0][1][1], yb1, 1024); SCHED(); \
        KE_M(XA, XB, 0, 1, 1); SCHED(); RD128(YA[0][1], ya0, 2048); SCHED(); \
        KE_M(XA, XB, 0, 1, 2); SCHED(); TRRD(YB[0][2][0], yb2, 0); SCHED(); \
        KE_M(XA, XB, 0, 1, 3); SCHED(); TRRD(YB[0][2][1], yb2, 1024); SCHED(); \
        KE_M(XA, XB, 0, 2, 0); SCHED(); RD128(YA[0][2], ya0, 4096); SCHED(); \
        KE_M(XA, XB, 0, 2, 1); SCHED(); TRRD(YB[0][3][0], yb3, 0); SCHED(); \
        KE_M(XA, XB, 0, 2, 2); SCHED(); TRRD(YB[0][3][1], yb3, 1024); SCHED(); \
        KE_M(XA, XB, 0, 2, 3); SCHED(); RD128(YA[0][3], ya0, 6144); SCHED(); \
        KE_M(XA, XB, 0, 3, 0); SCHED(); TRRD(YB[1][0][0], yb0, 8192); SCHED(); \
        KE_M(XA, XB, 0, 3, 1); SCHED(); TRRD(YB[1][0][1], yb0, 9216); SCHED(); \
        KE_M(XA, XB, 0, 3, 2); SCHED(); RD128(YA[1][0], ya1, 0); SCHED(); \
        KE_M(XA, XB, 0, 3, 3); SCHED(); TRRD(YB[1][1][0], yb1, 8192); SCHED(); \
        KE_M(XA, XB, 1, 0, 0); SCHED(); TRRD(YB[1][1][1], yb1, 9216); SCHED(); \
        KE_M(XA, XB, 1, 0, 1); SCHED(); RD128(YA[1][1], ya1, 2048); SCHED(); \
        KE_M(XA, XB, 1, 0, 2); SCHED(); TRRD(YB[1][2][0], yb2, 8192); SCHED(); \
        KE_M(XA, XB, 1, 0, 3); SCHED(); TRRD(YB[1][2][1], yb2, 9216); SCHED(); \
        KE_M(XA, XB, 1, 1, 0); SCHED(); RD128(YA[1][2], ya1, 4096); SCHED(); \
        KE_M(XA, XB, 1, 1, 1); SCHED(); TRRD(YB[1][3][0], yb3, 8192); SCHED(); \
        KE_M(XA, XB, 1, 1, 2); SCHED(); TRRD(YB[1][3][1], yb3, 9216); SCHED(); \
        KE_M(XA, XB, 1, 1, 3); SCHED(); RD128(YA[1][3], ya1, 6144); SCHED(); \
        KE_M(XA, XB, 1, 2, 0); SCHED(); SCHED(); \
        KE_M(XA, XB, 1, 2, 1); SCHED(); SCHED(); \
        KE_M(XA, XB, 1, 2, 2); SCHED(); SCHED(); \
        KE_M(XA, XB, 1, 2, 3); SCHED(); SCHED(); \
        KE_M(XA, XB, 1, 3, 0); SCHED(); SCHED(); \
        KE_M(XA, XB, 1, 3, 1); SCHED(); SCHED(); \
        KE_M(XA, XB, 1, 3, 2); SCHED(); SCHED(); \
        KE_M(XA, XB, 1, 3, 3); SCHED(); SCHED(); } while (0)
#define KE_S12(XA, XB, YA, YB, ya0, ya1, yb0, yb1, yb2, yb3) do { \
        KE_M(XA, XB, 0, 0, 0); SCHED(); TRRD(YB[0][0][0], yb0, 0); SCHED(); \
        KE_M(XA, XB, 0, 0, 1); SCHED(); TRRD(YB[0][0][1], yb0, 1024); SCHED(); \
        KE_M(XA, XB, 0, 0, 2); SCHED(); RD128(YA[0][0], ya0, 0); SCHED(); \
        KE_M(XA, XB, 0, 0, 3); SCHED(); TRRD(YB[0][1][0], yb1, 0); SCHED(); \
        KE_M(XA, XB, 0, 1, 0); SCHED(); TRRD(YB[0][1][1], yb1, 1024); SCHED(); \
        KE_M(XA, XB, 0, 1, 1); SCHED(); RD128(YA[0][1], ya0, 2048); SCHED(); \
        KE_M(XA, XB, 0, 1, 2); SCHED(); TRRD(YB[0][2][0], yb2, 0); SCHED(); \
        KE_M(XA, XB, 0, 1, 3); SCHED(); TRRD(YB[0][2][1], yb2, 1024); SCHED(); \
        KE_M(XA, XB, 0, 2, 0); SCHED(); TRRD(YB[0][3][0], yb3, 0); SCHED(); \
        KE_M(XA, XB, 0, 2, 1); SCHED(); TRRD(YB[0][3][1], yb3, 1024); SCHED(); \
        KE_M(XA, XB, 0, 2, 2); SCHED(); TRRD(YB[1][0][0], yb0, 8192); SCHED(); \
        KE_M(XA, XB, 0, 2, 3); SCHED(); TRRD(YB[1][0][1], yb0, 9216); SCHED(); \
        KE_M(XA, XB, 0, 3, 0); SCHED(); RD128(YA[1][0], ya1, 0); SCHED(); \
        KE_M(XA, XB, 0, 3, 1); SCHED(); TRRD(YB[1][1][0], yb1, 8192); SCHED(); \
        KE_M(XA, XB, 0, 3, 2); SCHED(); TRRD(YB[1][1][1], yb1, 9216); SCHED(); \
        KE_M(XA, XB, 0, 3, 3); SCHED(); RD128(YA[1][1], ya1, 2048); SCHED(); \
        KE_M(XA, XB, 1, 0, 0); SCHED(); TRRD(YB[1][2][0], yb2, 8192); SCHED(); \
        KE_M(XA, XB, 1, 0, 1); SCHED(); TRRD(YB[1][2][1], yb2, 9216); SCHED(); \
        KE_M(XA, XB, 1, 0, 2); SCHED(); TRRD(YB[1][3][0], yb3, 8192); SCHED(); \
        KE_M(XA, XB, 1, 0, 3); SCHED(); TRRD(YB[1][3][1], yb3, 9216); SCHED(); \
        KE_M(XA, XB, 1, 1, 0); SCHED(); SCHED(); \
        KE_M(XA, XB, 1, 1, 1); SCHED(); SCHED(); \
        KE_M(XA, XB, 1, 1, 2); SCHED(); SCHED(); \
        KE_M(XA, XB, 1, 1, 3); SCHED(); SCHED(); \
        KE_M(XA, XB, 1, 2, 0); SCHED(); SCHED(); \
        KE_M(XA, XB, 1, 2, 1); SCHED(); SCHED(); \
        KE_M(XA, XB, 1, 2, 2); SCHED(); SCHED(); \
        KE_M(XA, XB, 1, 2, 3); SCHED(); SCHED(); \
        KE_M(XA, XB, 1, 3, 0); SCHED(); SCHED(); \
        KE_M(XA, XB, 1, 3, 1); SCHED(); SCHED(); \
        KE_M(XA, XB, 1, 3, 2); SCHED(); SCHED(); \
        KE_M(XA, XB, 1, 3, 3); SCHED(); SCHED(); } while (0)
#define KE_S22(XA, XB, YA, YB, ya0, ya1, yb0, yb1, yb2, yb3) do { \
        KE_M(XA, XB, 0, 0, 0); SCHED(); TRRD(YB[0][0][0], yb0, 0); TRRD(YB[0][0][1], yb0, 1024); SCHED(); \
        KE_M(XA, XB, 0, 0, 1); SCHED(); RD128(YA[0][0], ya0, 0); TRRD(YB[0][1][0], yb1, 0); SCHED(); \
        KE_M(XA, XB, 0, 0, 2); SCHED(); TRRD(YB[0][1][1], yb1, 1024); RD128(YA[0][1], ya0, 2048); SCHED(); \
        KE_M(XA, XB, 0, 0, 3); SCHED(); TRRD(YB[0][2][0], yb2, 0); TRRD(YB[0][2][1], yb2, 1024); SCHED(); \
        KE_M(XA, XB, 0, 1, 0); SCHED(); TRRD(YB[0][3][0], yb3, 0); SCHED(); \
        KE_M(XA, XB, 0, 1, 1); SCHED(); TRRD(YB[0][3][1], yb3, 1024); SCHED(); \
        KE_M(XA, XB, 0, 1, 2); SCHED(); TRRD(YB[1][0][0], yb0, 8192); SCHED(); \
        KE_M(XA, XB, 0, 1, 3); SCHED(); TRRD(YB[1][0][1], yb0, 9216); SCHED(); \
        KE_M(XA, XB, 1, 0, 0); SCHED(); RD128(YA[1][0], ya1, 0); SCHED(); \
        KE_M(XA, XB, 1, 0, 1); SCHED(); TRRD(YB[1][1][0], yb1, 8192); SCHED(); \
        KE_M(XA, XB, 1, 0, 2); SCHED(); TRRD(YB[1][1][1], yb1, 9216); SCHED(); \
        KE_M(XA, XB, 1, 0, 3); SCHED(); RD128(YA[1][1], ya1, 2048); SCHED(); \
        KE_M(XA, XB, 1, 1, 0); SCHED(); TRRD(YB[1][2][0], yb2, 8192); SCHED(); \
        KE_M(XA, XB, 1, 1, 1); SCHED(); TRRD(YB[1][2][1], yb2, 9216); SCHED(); \
        KE_M(XA, XB, 1, 1, 2); SCHED(); TRRD(YB[1][3][0], yb3, 8192); SCHED(); \
        KE_M(XA, XB, 1, 1, 3); SCHED(); TRRD(YB[1][3][1], yb3, 9216); SCHED(); } while (0)
#define KE_S20(XA, XB, YA, YB, ya0, ya1, yb0, yb1, yb2, yb3) do { \
        KE_M(XA, XB, 0, 0, 0); SCHED(); SCHED(); \
        KE_M(XA, XB, 0, 0, 1); SCHED(); SCHED(); \
        KE_M(XA, XB, 0, 0, 2); SCHED(); SCHED(); \
        KE_M(XA, XB, 0, 0, 3); SCHED(); SCHED(); \
        KE_M(XA, XB, 0, 1, 0); SCHED(); SCHED(); \
        KE_M(XA, XB, 0, 1, 1); SCHED(); SCHED(); \
        KE_M(XA, XB, 0, 1, 2); SCHED(); SCHED(); \
        KE_M(XA, XB, 0, 1, 3); SCHED(); SCHED(); \
        KE_M(XA, XB, 1, 0, 0); SCHED(); SCHED(); \
        KE_M(XA, XB, 1, 0, 1); SCHED(); SCHED(); \
        KE_M(XA, XB, 1, 0, 2); SCHED(); SCHED(); \
        KE_M(XA, XB, 1, 0, 3); SCHED(); SCHED(); \
        KE_M(XA, XB, 1, 1, 0); SCHED(); SCHED(); \
        KE_M(XA, XB, 1, 1, 1); SCHED(); SCHED(); \
        KE_M(XA, XB, 1, 1, 2); SCHED(); SCHED(); \
        KE_M(XA, XB, 1, 1, 3); SCHED(); SCHED(); } while (0)

struct G2Load { unsigned vA1, vA2, vB0, vB1; const char *cA, *cB; };
#define KD_B4(pb, pl) do { GLDS((pb) + g2.vB0, (pl) + 16384 + lw * 4096); GLDS((pb) + g2.vB0 + (size_t)4 * DM * 2, (pl) + 16384 + lw * 4096 + 1024); \
        GLDS((pb) + g2.vB1, (pl) + 16384 + lw * 4096 + 2048); GLDS((pb) + g2.vB1 + (size_t)4 * DM * 2, (pl) + 16384 + lw * 4096 + 3072); } while (0)
#define KD_ISSUE1A(s_, slot) do { const char* _a = g2.cA + (size_t)(KC_N1 * ks + (s_)) * 128 + g2.vA1; LAS unsigned char* _l = lds + (slot) * KB_STAGE; \
        GLDS(_a, _l + lw * 4096); GLDS(_a + (size_t)8 * FF * 2, _l + lw * 4096 + 1024); GLDS(_a + (size_t)16 * FF * 2, _l + lw * 4096 + 2048); GLDS(_a + (size_t)24 * FF * 2, _l + lw * 4096 + 3072); } while (0)
#define KD_ISSUE1B(s_, slot) do { const char* _b = g2.cB + (size_t)(KC_N1 * ks + (s_)) * 64 * DM * 2; KD_B4(_b, lds + (slot) * KB_STAGE); } while (0)
#define KD_ISSUE1(s_, slot) do { KD_ISSUE1A(s_, slot); KD_ISSUE1B(s_, slot); } while (0)
#define KD_ISSUE2(s_, slot) do { const int _kt = KC_N1 + (s_); const char* _a = g2.cA + (size_t)_kt * 128 + g2.vA2; const char* _b = g2.cB + (size_t)_kt * 64 * DM * 2; LAS unsigned char* _l = lds + (slot) * KB_STAGE; \
        GLDS(_a, _l + ks * 8192 + lw * 2048); GLDS(_a + (size_t)8 * FF * 2, _l + ks * 8192 + lw * 2048 + 1024); KD_B4(_b, _l); } while (0)
__device__ __forceinline__ G2Load gemm2_addr(const Params& P, int ks, int pm, int pn, int lw, int lane) {
    G2Load g2;
    const unsigned swA = (unsigned)(8 * ((lane & 7) ^ ((lane >> 3) & 7)) * 2);
    g2.vA1 = (unsigned)((lw * 32 + (lane >> 3)) * FF * 2) + swA;
    g2.vA2 = (unsigned)((ks * 64 + lw * 16 + (lane >> 3)) * FF * 2) + swA;
    g2.vB0 = (unsigned)(((lw * 16 + (lane >> 4)) * DM + 8 * ((lane & 15) ^ (((lane >> 4) & 3) << 2))) * 2);
    g2.vB1 = (unsigned)(((lw * 16 + 8 + (lane >> 4)) * DM + 8 * ((lane & 15) ^ ((((lane >> 4) & 3) << 2) | 2))) * 2);
    g2.cA = (const char*)P.hbuf + (size_t)pm * 128 * FF * 2;
    g2.cB = (const char*)P.wd + (size_t)pn * 128 * 2;
    return g2;
}
__device__ __forceinline__ void gemm2_loader(const Params& P, LAS unsigned char* lds, const G2Load& g2, int ks, int pm, int pn, int lw, int lane) {
    const int spare = (1 - ks) * 8192;
    u32x4 ey[4], ex[4]; unsigned es;
#define KD_LDY(i) do { const int row = (lw * 4 + (i)) * 8 + (lane >> 3), ck = (lane & 7) ^ (((row >> 1) & 3) << 1); \
        const char* _p = (const char*)P.y2T + ((size_t)(pn * 128 + row) * SEQ + pm * 128 + ks * 64 + 8 * ck) * 2; \
        asm volatile("global_load_dwordx4 %0, %1, off" : "=&v"(ey[i]) : "v"(_p) : "memory"); } while (0)
#define KD_LDX(i) do { const int xr = (lw * 4 + (i)) * 4 + (lane >> 4), xc = (lane & 15) ^ (xr & 15); \
        const char* _p = (const char*)P.x + ((size_t)(pm * 128 + ks * 64 + xr) * DM + pn * 128 + 8 * xc) * 2; \
        asm volatile("global_load_dwordx4 %0, %1, off" : "=&v"(ex[i]) : "v"(_p) : "memory"); } while (0)
#define KD_LDS() do { const char* _p = (const char*)P.stats + (size_t)pm * 128 * 8 + (lw * 64 + lane) * 4; \
        asm volatile("global_load_dword %0, %1, off" : "=&v"(es) : "v"(_p) : "memory"); } while (0)
#define KD_PUT() do { asm volatile("" : "+v"(ey[0]), "+v"(ey[1]), "+v"(ey[2]), "+v"(ey[3]), "+v"(ex[0]), "+v"(ex[1]), "+v"(ex[2]), "+v"(ex[3]), "+v"(es)); \
        _Pragma("unroll") for (int i = 0; i < 4; ++i) { \
            *(LAS u32x4*)(lds + (2 + (lw >> 1)) * KB_STAGE + spare + ((lw & 1) * 4 + i) * 1024 + lane * 16) = ey[i]; \
            *(LAS u32x4*)(lds + (lw >> 1) * KB_STAGE + spare + ((lw & 1) * 4 + i) * 1024 + lane * 16) = ex[i]; } \
        *(LAS unsigned*)(lds + 4 * KB_STAGE + spare + 1024 + (lw * 64 + lane) * 4) = es; } while (0)
        KD_ISSUE1A(0, 0); KD_ISSUE1A(1, 1); KD_ISSUE1A(2, 2); KD_ISSUE1A(3, 3);
        WAIT_V(12); BAR();
        WAIT_V(8); BAR(); KD_ISSUE1(4, 4);
        WAIT_V(12); BAR(); KD_ISSUE1(5, 0);
        WAIT_V(16); BAR(); KD_LDY(0); KD_ISSUE1(6, 1);
        WAIT_V(17); BAR(); KD_ISSUE1(7, 2);
        WAIT_V(17); BAR(); KD_LDX(0); KD_ISSUE1(8, 3);
        WAIT_V(17); BAR(); KD_ISSUE1(9, 4);
        WAIT_V(17); BAR(); KD_LDY(1); KD_ISSUE1(10, 0);
        WAIT_V(17); BAR(); KD_ISSUE1(11, 1);
        WAIT_V(17); BAR(); KD_LDX(1); KD_ISSUE1(12, 2);
        WAIT_V(17); BAR(); KD_ISSUE1(13, 3);
        WAIT_V(17); BAR(); KD_LDY(2); KD_ISSUE1(14, 4);
        WAIT_V(17); BAR(); KD_ISSUE1(15, 0);
        WAIT_V(17); BAR(); KD_LDX(2); KD_ISSUE1(16, 1);
        WAIT_V(17); BAR(); KD_ISSUE1(17, 2);
        WAIT_V(17); BAR(); KD_LDY(3); KD_ISSUE1(18, 3);
        WAIT_V(17); BAR(); KD_ISSUE1(19, 4);
        WAIT_V(17); BAR(); KD_LDX(3); KD_ISSUE1(20, 0);
        WAIT_V(17); BAR(); KD_ISSUE1(21, 1);
        WAIT_V(17); BAR(); KD_LDS(); KD_ISSUE1(22, 2);
        WAIT_V(17); BAR(); KD_ISSUE2(23, 3);
        WAIT_V(15); BAR(); KD_ISSUE2(24, 4);
        WAIT_V(12); BAR(); KD_ISSUE2(25, 0);
        WAIT_V(12); BAR(); KD_ISSUE2(26, 1); BAR();
        WAIT_V(12); BAR(); KD_PUT(); KD_ISSUE2(27, 2); WAIT_L(0);
        WAIT_V(12); BAR(); KD_ISSUE2(28, 3);
        WAIT_V(12); BAR(); KD_ISSUE2(29, 4);
        WAIT_V(12); BAR(); KD_ISSUE2(30, 0);
        WAIT_V(12); BAR(); KD_ISSUE2(31, 1);
        WAIT_V(12); BAR(); KD_ISSUE2(32, 2);
        WAIT_V(12); BAR();
        WAIT_V(6); BAR();
        WAIT_V(0); BAR();
#undef KD_LDY
#undef KD_LDX
#undef KD_LDS
#undef KD_PUT
}
__device__ __forceinline__ void gemm2_mult(const Params& P, LAS unsigned char* lds, int ks, int pm, int pn, int wid, int lane, u64 tag, int& _sti) {
    const int tid = threadIdx.x, spare = (1 - ks) * 8192;
    const int wr = wid >> 1, wc = wid & 1, rg = (wr != ks) ? 1 : 0, fr = lane & 15, fq = lane >> 4, q = (lane & 15) >> 2, p = lane & 3;
    const unsigned ldsb = (unsigned)(uintptr_t)lds;
    const int a1row = wr * 64 + fr, a2row = ks * 64 + rg * 32 + fr;
    const unsigned a1k0 = ldsb + a1row * 128 + 16 * ((0 + fq) ^ (fr & 7)), a1k1 = ldsb + a1row * 128 + 16 * ((4 + fq) ^ (fr & 7));
    const unsigned a2k0 = ldsb + a2row * 128 + 16 * ((0 + fq) ^ (fr & 7)), a2k1 = ldsb + a2row * 128 + 16 * ((4 + fq) ^ (fr & 7));
    unsigned bo[4];
#pragma unroll
    for (int n = 0; n < 4; ++n) bo[n] = ldsb + 16384 + 256 * (8 * fq + q) + 16 * ((8 * wc + 2 * n + (p >> 1)) ^ (4 * q + 2 * (fq & 1))) + 8 * (p & 1);
#define KE_M(Af, Bf, kk, m, n) acc[m][n] = __builtin_amdgcn_mfma_f32_16x16x32_bf16(CAT(Bf[kk][n][0], Bf[kk][n][1]), Af[kk][m], acc[m][n], 0, 0, 0)
#define KE_WAITL(Af, Bf, NM) do { asm volatile("s_waitcnt lgkmcnt(0)" : "+v"(Bf[0][0][0]), "+v"(Bf[0][0][1]), "+v"(Bf[0][1][0]), "+v"(Bf[0][1][1]), "+v"(Bf[0][2][0]), "+v"(Bf[0][2][1]), "+v"(Bf[0][3][0]), "+v"(Bf[0][3][1]), \
        "+v"(Bf[1][0][0]), "+v"(Bf[1][0][1]), "+v"(Bf[1][1][0]), "+v"(Bf[1][1][1]), "+v"(Bf[1][2][0]), "+v"(Bf[1][2][1]), "+v"(Bf[1][3][0]), "+v"(Bf[1][3][1]) :: "memory"); \
        asm volatile("" : "+v"(Af[0][0]), "+v"(Af[0][1]), "+v"(Af[1][0]), "+v"(Af[1][1])); if ((NM) == 4) asm volatile("" : "+v"(Af[0][2]), "+v"(Af[0][3]), "+v"(Af[1][2]), "+v"(Af[1][3])); } while (0)
    f32x4 acc[4][4];
#pragma unroll
    for (int m = 0; m < 4; ++m)
#pragma unroll
        for (int n = 0; n < 4; ++n) acc[m][n] = (f32x4){0.f, 0.f, 0.f, 0.f};
    bf16x8 PA[2][4], QA[2][4]; bf16x4 PB[2][4][2], QB[2][4][2];
    int so = 0;
#define KD_ADDR(AK0, AK1) const unsigned _a0 = (AK0) + so, _a1 = (AK1) + so, _b0 = bo[0] + so, _b1 = bo[1] + so, _b2 = bo[2] + so, _b3 = bo[3] + so
#define KD_NEXT() so = (so == 4 * KB_STAGE) ? 0 : so + KB_STAGE
    BAR();
    ST();
    { KD_ADDR(a1k0, a1k1); KE_S01(QA, QB, PA, PB, _a0, _a1, _b0, _b1, _b2, _b3); KD_NEXT(); }
#define KE_STEP(MAC, NM, AK0, AK1, XA, XB, YA, YB) do { KD_ADDR(AK0, AK1); unsigned _u0 = _a0, _u1 = _a1, _v0 = _b0, _v1 = _b1, _v2 = _b2, _v3 = _b3; \
        asm volatile("" : "+v"(_u0), "+v"(_u1), "+v"(_v0), "+v"(_v1), "+v"(_v2), "+v"(_v3)); \
        KE_WAITL(XA, XB, NM); BAR(); SCHED(); MAC(XA, XB, YA, YB, _u0, _u1, _v0, _v1, _v2, _v3); KD_NEXT(); } while (0)
    const int erow = pm * 128 + ks * 64 + rg * 32 + fr;
    const int ecol = pn * 128 + wc * 64 + 16 * (fq & 1) + 8 * (fq >> 1);
    for (int s = 0; s < 22; s += 2) {
        KE_STEP(KE_S11, 4, a1k0, a1k1, PA, PB, QA, QB);
        KE_STEP(KE_S11, 4, a1k0, a1k1, QA, QB, PA, PB);
    }
    ST();
    KE_STEP(KE_S12, 4, a2k0, a2k1, PA, PB, QA, QB);
    const rsrc_t rp = mk_rsrc(P.part, 256 * 16384);
    LAS unsigned* cnt = (LAS unsigned*)(lds + 4 * KB_STAGE + spare);
    {
        LAS f32x4* ho = (LAS f32x4*)(lds + wc * KB_STAGE + spare);
        if (rg == 0) {
#pragma unroll
            for (int m = 0; m < 2; ++m)
#pragma unroll
                for (int n = 0; n < 4; ++n) ho[(m * 4 + n) * 64 + lane] = acc[2 + m][n];
            if (tid == (ks << 7)) *cnt = 0u;
        } else {
#pragma unroll
            for (int m = 0; m < 4; ++m)
#pragma unroll
                for (int h = 0; h < 2; ++h) {
                    const f32x4 a0 = acc[m][2 * h], a1 = acc[m][2 * h + 1];
                    st16(rp, blockIdx.x * 16384u + (unsigned)(wc * 8192 + (m * 2 + h) * 1024 + lane * 16),
                         (u32x4){pack_bf16(a0[0], a0[1]), pack_bf16(a0[2], a0[3]), pack_bf16(a1[0], a1[1]), pack_bf16(a1[2], a1[3])});
                }
        }
        WAIT_L(0); BAR();
        if (rg == 1) {
#pragma unroll
            for (int m = 0; m < 2; ++m)
#pragma unroll
                for (int n = 0; n < 4; ++n) acc[m][n] = ho[(m * 4 + n) * 64 + lane];
        }
    }
    KE_STEP(KE_S22, 2, a2k0, a2k1, QA, QB, PA, PB);
    KE_STEP(KE_S22, 2, a2k0, a2k1, PA, PB, QA, QB);
    KE_STEP(KE_S22, 2, a2k0, a2k1, QA, QB, PA, PB);
    KE_STEP(KE_S22, 2, a2k0, a2k1, PA, PB, QA, QB);
    if (rg == 1) {
        WAIT_V(0);
        if (lane == 0) {
            const unsigned n = __hip_atomic_fetch_add(cnt, 1u, __ATOMIC_RELAXED, __HIP_MEMORY_SCOPE_WORKGROUP);
            if (n == 1u) signal(P.sync + SY_P + blockIdx.x * 16, tag);
        }
    }
    KE_STEP(KE_S22, 2, a2k0, a2k1, QA, QB, PA, PB);
    KE_STEP(KE_S22, 2, a2k0, a2k1, PA, PB, QA, QB);
    KE_STEP(KE_S22, 2, a2k0, a2k1, QA, QB, PA, PB);
    u64 pf;
    {
        const u64* pfl = P.sync + SY_P + (blockIdx.x ^ 1) * 16;
        asm volatile("global_load_dwordx2 %0, %1, off sc1" : "=&v"(pf) : "v"(pfl) : "memory");
    }
    KE_STEP(KE_S22, 2, a2k0, a2k1, PA, PB, QA, QB);
    KE_STEP(KE_S22, 2, a2k0, a2k1, QA, QB, PA, PB);
    ST();
    asm volatile("s_waitcnt vmcnt(0)" : "+v"(pf) :: "memory");
    if (!__all(pf == tag)) poll_words(P.sync + SY_P + (blockIdx.x ^ 1) * 16, true, tag);
    asm volatile("" ::: "memory");
    u32x4 pin[2][2];
#pragma unroll
    for (int m = 0; m < 2; ++m)
#pragma unroll
        for (int h = 0; h < 2; ++h) {
            const unsigned o = (blockIdx.x ^ 1) * 16384u + (unsigned)(wc * 8192 + ((2 * rg + m) * 2 + h) * 1024 + lane * 16);
            asm volatile("buffer_load_dwordx4 %0, %1, %2, 0 offen sc1" : "=&v"(pin[m][h]) : "v"(o), "s"(rp) : "memory");
        }
    KE_WAITL(PA, PB, 2); SCHED();
    KE_S20(PA, PB, QA, QB, 0, 0, 0, 0, 0, 0);
    ST();
    bf16x4 yv[2][2][2]; u32x4 xv[2][2]; f32x2 st[2];
#pragma unroll
    for (int m = 0; m < 2; ++m) {
        st[m] = *(const LAS f32x2*)(lds + 4 * KB_STAGE + spare + 1024 + (ks * 64 + rg * 32 + 16 * m + fr) * 8);
#pragma unroll
        for (int h = 0; h < 2; ++h) {
            const int xr = 16 * m + fr, xc = 8 * wc + 4 * h + 2 * (fq & 1) + (fq >> 1);
            xv[m][h] = *(const LAS u32x4*)(lds + rg * KB_STAGE + spare + xr * 256 + 16 * (xc ^ (xr & 15)));
        }
    }
#pragma unroll
    for (int m = 0; m < 2; ++m)
#pragma unroll
        for (int h = 0; h < 2; ++h) {
            const int crow = 32 * h + 16 * (fq & 1) + 8 * (fq >> 1) + q;
            const int tck = (rg * 32 + 16 * m) / 8 + (p >> 1);
            const unsigned yb = ldsb + (2 + wc) * KB_STAGE + spare + crow * 128 + 8 * (p & 1);
            TRRD(yv[m][h][0], yb + 16 * (tck ^ (((crow >> 1) & 3) << 1)), 0);
            TRRD(yv[m][h][1], yb + 16 * (tck ^ ((((crow + 4) >> 1) & 3) << 1)), 512);
        }
    asm volatile("s_waitcnt vmcnt(0) lgkmcnt(0)" : "+v"(pin[0][0]), "+v"(pin[0][1]), "+v"(pin[1][0]), "+v"(pin[1][1]), "+v"(yv[0][0][0]), "+v"(yv[0][0][1]), "+v"(yv[0][1][0]), "+v"(yv[0][1][1]),
                 "+v"(yv[1][0][0]), "+v"(yv[1][0][1]), "+v"(yv[1][1][0]), "+v"(yv[1][1][1]) :: "memory");
    const rsrc_t ro = mk_rsrc(P.out, SEQ * DM * 2);
#pragma unroll
    for (int m = 0; m < 2; ++m) {
        const float rs = st[m][1], mrs = -st[m][0] * rs;
#pragma unroll
        for (int h = 0; h < 2; ++h) {
            const u32x4 w = pin[m][h];
            const f32x4 a0 = acc[m][2 * h] + (f32x4){bf_lo(w[0]), bf_hi(w[0]), bf_lo(w[1]), bf_hi(w[1])};
            const f32x4 a1 = acc[m][2 * h + 1] + (f32x4){bf_lo(w[2]), bf_hi(w[2]), bf_lo(w[3]), bf_hi(w[3])};
            float o[8];
#pragma unroll
            for (int j = 0; j < 4; ++j) {
                const auto sw = __builtin_amdgcn_permlane16_swap(__float_as_uint(a0[j]), __float_as_uint(a1[j]), false, false);
                o[j] = __uint_as_float(sw[0]); o[4 + j] = __uint_as_float(sw[1]);
            }
#pragma unroll
            for (int j = 0; j < 8; ++j) {
                const unsigned xw = xv[m][h][j >> 1];
                const float xf = (j & 1) ? bf_hi(xw) : bf_lo(xw);
                const float yf = __uint_as_float((unsigned)(u16)yv[m][h][j >> 2][j & 3] << 16);
                o[j] += xf + (xf * rs + mrs) * yf;
            }
            st16(ro, (unsigned)(((erow + 16 * m) * DM + ecol + 32 * h) * 2), (u32x4){pack_bf16(o[0], o[1]), pack_bf16(o[2], o[3]), pack_bf16(o[4], o[5]), pack_bf16(o[6], o[7])});
        }
    }
    ST();
#undef KE_M
#undef KE_WAITL
#undef KE_STEP
#undef KD_ADDR
#undef KD_NEXT
}

constexpr int PA_K = 0, PA_V = 17408, PA_T = 34816;
__global__ __launch_bounds__(512, 2) void fused_kernel(Params P) {
    extern __shared__ __attribute__((aligned(16))) unsigned char shm[];
    LAS unsigned char* lds = (LAS unsigned char*)shm;
    int _sti = 0;
    ST();
    const int tid = threadIdx.x, wid = __builtin_amdgcn_readfirstlane(tid >> 6), lane = tid & 63;
    const int b = blockIdx.x, xcd = b & 7, bi = b >> 3;
    const u64 tag = __hip_atomic_load((gu64*)(P.sync + SY_EPOCH), __ATOMIC_RELAXED, __HIP_MEMORY_SCOPE_AGENT) + 1ull;
    const bool is_gemm = bi < 28;
    const int pm1 = 4 * (xcd & 1) + (bi & 3), pn1 = 7 * (xcd >> 1) + (bi >> 2), cb = xcd * 4 + (bi - 28);
    {
        const int row = b * 8 + wid, cg = b & 15, rt = b >> 4;
        const u16* xr = P.x + (size_t)row * DM;
        const u32x4 a = ld16_stream(xr + lane * 8), bq = ld16_stream(xr + 512 + lane * 8);
#pragma unroll
        for (int i = 0; i < 2; ++i) {
            const int j = wid + 8 * i, kr = 128 * rt - 8 + 8 * j + (lane >> 3);
            __builtin_amdgcn_global_load_lds((const GAS unsigned*)(P.kin + (size_t)(kr < 0 ? 0 : kr) * DM + cg * 64 + (lane & 7) * 8), (LAS unsigned*)(lds + PA_K + j * 1024), 16, 0, 2);
            __builtin_amdgcn_global_load_lds((const GAS unsigned*)(P.vin + (size_t)(128 * rt + 8 * j + (lane >> 3)) * DM + cg * 64 + (lane & 7) * 8), (LAS unsigned*)(lds + PA_V + j * 1024), 16, 0, 2);
        }
        if (wid == 0) __builtin_amdgcn_global_load_lds((const GAS unsigned*)(P.kin + (size_t)(128 * rt + 120 + (lane >> 3)) * DM + cg * 64 + (lane & 7) * 8), (LAS unsigned*)(lds + PA_K + 16 * 1024), 16, 0, 2);
        const u32x2 wt = *(const u32x2*)(P.shk + (size_t)(cg * 64 + lane) * 4);
        float v[16];
#pragma unroll
        for (int i = 0; i < 4; ++i) { v[2 * i] = bf_lo(a[i]); v[2 * i + 1] = bf_hi(a[i]); v[8 + 2 * i] = bf_lo(bq[i]); v[9 + 2 * i] = bf_hi(bq[i]); }
        float s = 0.f;
#pragma unroll
        for (int i = 0; i < 16; ++i) s += v[i];
#pragma unroll
        for (int o = 32; o >= 1; o >>= 1) s += __shfl_xor(s, o);
        const float mean = s * (1.0f / DM);
        float q = 0.f;
#pragma unroll
        for (int i = 0; i < 16; ++i) { const float d = v[i] - mean; q += d * d; }
#pragma unroll
        for (int o = 32; o >= 1; o >>= 1) q += __shfl_xor(q, o);
        const float mb = bf_round(mean), vb = bf_round(q * (1.0f / DM));
        const float sb = bf_round(sqrtf(bf_round(vb + 1e-5f)));
        const float rs = 1.0f / sb;
        u32x4 oa, ob;
#pragma unroll
        for (int i = 0; i < 4; ++i) {
            oa[i] = pack_bf16(bf_round(v[2 * i] - mb) * rs, bf_round(v[2 * i + 1] - mb) * rs);
            ob[i] = pack_bf16(bf_round(v[8 + 2 * i] - mb) * rs, bf_round(v[9 + 2 * i] - mb) * rs);
        }
        const float w0 = bf_lo(wt[0]), w1 = bf_hi(wt[0]), w2 = bf_lo(wt[1]), w3 = bf_hi(wt[1]);
        WAIT_V(0);
        const rsrc_t rx = mk_rsrc(P.xn, SEQ * DM * 2);
        st16(rx, (unsigned)(row * DM * 2 + lane * 16), oa); st16(rx, (unsigned)(row * DM * 2 + 1024 + lane * 16), ob);
        st8(mk_rsrc(P.stats, SEQ * 8), lane == 0 ? (unsigned)(row * 8) : 0xfffffff0u, (u32x2){__float_as_uint(mb), __float_as_uint(rs)});
        BAR();
        float kf[19], kv[16];
#pragma unroll
        for (int i = 0; i < 19; ++i) kf[i] = bf_lo((unsigned)*(const LAS u16*)(lds + PA_K + (16 * wid + 5 + i) * 128 + lane * 2));
        if (rt == 0 && wid == 0) { kf[0] = 0.f; kf[1] = 0.f; kf[2] = 0.f; }
#pragma unroll
        for (int i = 0; i < 16; ++i) {
            const float vf = bf_lo((unsigned)*(const LAS u16*)(lds + PA_V + (16 * wid + i) * 128 + lane * 2));
            kv[i] = (kf[i] * w3 + kf[i + 1] * w2 + kf[i + 2] * w1 + kf[i + 3] * w0) * vf;
        }
#pragma unroll
        for (int j = 0; j < 2; ++j) {
            u32x4 o;
#pragma unroll
            for (int i = 0; i < 4; ++i) o[i] = pack_bf16(kv[8 * j + 7 - 2 * i], kv[8 * j + 6 - 2 * i]);
            *(LAS u32x4*)(lds + PA_T + lane * 256 + 16 * ((2 * wid + j) ^ (lane & 15))) = o;
        }
        WAIT_L(0); WAIT_V(0); BAR();
        if (tid == 0) signal(P.sync + SY_X + b, tag);
        const rsrc_t rk = mk_rsrc(P.kvT, SEQ * DM * 2);
#pragma unroll
        for (int i = 0; i < 2; ++i) {
            const int ch = 8 * wid + 4 * i + (lane >> 4), ck = lane & 15;
            st16(rk, (unsigned)(((cg * 64 + ch) * SEQ + 128 * rt + 8 * ck) * 2), *(const LAS u32x4*)(lds + PA_T + ch * 256 + 16 * (ck ^ (ch & 15))));
        }
    }
    ST();
    if (is_gemm) {
        const G1Addr ga = gemm1_addr(P, pm1, pn1);
        STAGE_B(SB(0, 0), ga.cG, 0); STAGE_B(SB(0, 1), ga.cU, 0); STAGE_B(SB(1, 0), ga.cG, 1); STAGE_B(SB(1, 1), ga.cU, 1);
        WAIT_V(8); BAR();
        if (tid == 0) signal(P.sync + SY_K + (b & 15) * 16 + (b >> 4), tag);
        if (wid == 0) { poll_words(P.sync + SY_X + pm1 * 32 + (lane & 31), true, tag); ACQUIRE(); }
        WAIT_V(0); BAR();
        ST();
        gemm1_block(P, lds, ga, pm1, pn1, _sti);
    } else {
        WAIT_V(0); BAR();
        if (tid == 0) signal(P.sync + SY_K + (b & 15) * 16 + (b >> 4), tag);
        if (wid == 0) { poll_words(P.sync + SY_K + (cb >> 1) * 16 + (lane & 15), true, tag); ACQUIRE(); }
        BAR();
        ST();
        ST();
        conv_block(P, lds, cb, _sti);
        ST();
    }
    const int ks = xcd & 1, pm2 = 8 * ((xcd >> 1) & 1) + (bi & 7), pn2 = 4 * (xcd >> 2) + (bi >> 3);
    BAR();
    if (wid >= 4) {
        const int lw = wid - 4;
        const G2Load g2 = gemm2_addr(P, ks, pm2, pn2, lw, lane);
        KD_ISSUE1B(0, 0); KD_ISSUE1B(1, 1); KD_ISSUE1B(2, 2); KD_ISSUE1B(3, 3);
        WAIT_V(16); BAR();
        BAR();
        gemm2_loader(P, lds, g2, ks, pm2, pn2, lw, lane);
        if (b == 0 && wid == 4) {
#pragma unroll
            for (int i = 0; i < 4; ++i) poll_words(P.sync + SY_X + i * 64 + lane, true, tag);
            if (lane == 0) signal(P.sync + SY_EPOCH, tag);
        }
    } else {
        WAIT_V(0); BAR();
        if (tid == 0) signal(is_gemm ? P.sync + SY_H + pm1 * 32 + pn1 : P.sync + SY_Y + cb, tag);
        if (wid == 0) {
            poll_words(lane < 28 ? P.sync + SY_H + (pm2 >> 1) * 32 + lane : P.sync + SY_Y + 4 * pn2 + (lane & 3), lane < 32, tag);
            ACQUIRE();
        }
        BAR();
        ST();
        gemm2_mult(P, lds, ks, pm2, pn2, wid, lane, tag, _sti);
    }
}

extern "C" void kernel_launch(void* const* d_in, const int* in_sizes, int n_in, void* d_out, int out_size, void* d_ws, size_t ws_size, hipStream_t stream) {
    unsigned char* ws = (unsigned char*)d_ws;
    const size_t MB = 1u << 20;
    Params P;
    P.x = (const u16*)d_in[0]; P.kin = (const u16*)d_in[1]; P.vin = (const u16*)d_in[2]; P.shk = (const u16*)d_in[3]; P.ssmk = (const u16*)d_in[4]; P.ssmD = (const u16*)d_in[5];
    P.wg = (const u16*)d_in[7]; P.wu = (const u16*)d_in[8]; P.wd = (const u16*)d_in[9];
    P.out = (u16*)d_out;
    P.xn = (u16*)(ws + 0 * MB); P.kvT = (u16*)(ws + 4 * MB); P.y2T = (u16*)(ws + 8 * MB); P.hbuf = (u16*)(ws + 16 * MB);
    P.stats = (f32x2*)(ws + 12 * MB); P.part = (float*)(ws + 32 * MB); P.sync = (u64*)(ws + 13 * MB); P.stamps = (u64*)(ws + 64 * MB);
    (void)hipFuncSetAttribute((const void*)fused_kernel, hipFuncAttributeMaxDynamicSharedMemorySize, LDS_BYTES);
    fused_kernel<<<256, 512, LDS_BYTES, stream>>>(P);
}
```

```cpp
#include <hip/hip_runtime.h>
#include <hip/hip_bf16.h>
#include <stdint.h>

#define LAS __attribute__((address_space(3)))
#define GAS __attribute__((address_space(1)))
typedef unsigned short u16;
typedef short bf16x8 __attribute__((ext_vector_type(8)));
typedef short bf16x4 __attribute__((ext_vector_type(4)));
typedef float f32x4 __attribute__((ext_vector_type(4)));
typedef float f32x2 __attribute__((ext_vector_type(2)));
typedef unsigned u32x4 __attribute__((ext_vector_type(4)));
typedef unsigned u32x2 __attribute__((ext_vector_type(2)));
typedef __bf16 bf16x2_t __attribute__((ext_vector_type(2)));
typedef unsigned long long u64;
typedef GAS u64 gu64;

constexpr int SEQ = 2048, DM = 1024, FF = 3584;

__device__ __forceinline__ unsigned pack_bf16(float lo, float hi) {
    f32x2 v = {lo, hi};
    return __builtin_bit_cast(unsigned, __builtin_convertvector(v, bf16x2_t));
}
__device__ __forceinline__ float bf_lo(unsigned u) { return __uint_as_float(u << 16); }
__device__ __forceinline__ float bf_hi(unsigned u) { return __uint_as_float(u & 0xffff0000u); }
__device__ __forceinline__ float bf_round(float f) { return bf_lo(pack_bf16(f, f)); }

__device__ __forceinline__ float wave_sum(float v) {
    v += __builtin_bit_cast(float, __builtin_amdgcn_update_dpp(0, __builtin_bit_cast(int, v), 0x111, 0xf, 0xf, true));
    v += __builtin_bit_cast(float, __builtin_amdgcn_update_dpp(0, __builtin_bit_cast(int, v), 0x112, 0xf, 0xf, true));
    v += __builtin_bit_cast(float, __builtin_amdgcn_update_dpp(0, __builtin_bit_cast(int, v), 0x114, 0xf, 0xf, true));
    v += __builtin_bit_cast(float, __builtin_amdgcn_update_dpp(0, __builtin_bit_cast(int, v), 0x118, 0xf, 0xf, true));
    v += __builtin_bit_cast(float, __builtin_amdgcn_update_dpp(0, __builtin_bit_cast(int, v), 0x142, 0xa, 0xf, true));
    v += __builtin_bit_cast(float, __builtin_amdgcn_update_dpp(0, __builtin_bit_cast(int, v), 0x143, 0xc, 0xf, true));
    return __builtin_bit_cast(float, __builtin_amdgcn_readlane(__builtin_bit_cast(int, v), 63));
}
typedef __amdgpu_buffer_rsrc_t rsrc_t;
__device__ __forceinline__ rsrc_t mk_rsrc(const void* base, unsigned bytes) { return __builtin_amdgcn_make_buffer_rsrc((void*)base, 0, bytes, 0x00020000); }
__device__ __forceinline__ void st16(rsrc_t r, unsigned off, u32x4 v) { __builtin_amdgcn_raw_buffer_store_b128(v, r, off, 0, 16); }
__device__ __forceinline__ void st8(rsrc_t r, unsigned off, u32x2 v) { __builtin_amdgcn_raw_buffer_store_b64(v, r, off, 0, 16); }
__device__ __forceinline__ u32x4 ld16_stream(const void* p) { return __builtin_nontemporal_load((const u32x4*)p); }

#define WAIT_V(n) asm volatile("s_waitcnt vmcnt(" #n ")" ::: "memory")
#define WAIT_L(n) asm volatile("s_waitcnt lgkmcnt(" #n ")" ::: "memory")
#define BAR() __builtin_amdgcn_s_barrier()
#define SCHED() __builtin_amdgcn_sched_barrier(0)
#define GLDS(gptr, ldsptr) __builtin_amdgcn_global_load_lds((const GAS unsigned*)(gptr), (LAS unsigned*)(ldsptr), 16, 0, 0)
#define GLDS_DEV(gptr, ldsptr) __builtin_amdgcn_global_load_lds((const GAS unsigned*)(gptr), (LAS unsigned*)(ldsptr), 16, 0, 16)
#define TRRD(dst, addr, off) asm volatile("ds_read_b64_tr_b16 %0, %1 offset:%c2" : "=&v"(dst) : "v"(addr), "i"(off) : "memory")
#define RD128(dst, addr, off) asm volatile("ds_read_b128 %0, %1 offset:%c2" : "=&v"(dst) : "v"(addr), "i"(off) : "memory")
#define CAT(lo, hi) __builtin_shufflevector(lo, hi, 0, 1, 2, 3, 4, 5, 6, 7)

#define ST()

struct Params {
    const u16 *x, *kin, *vin, *shk, *ssmk, *ssmD, *wg, *wu, *wd;
    u16 *out, *xn, *kvT, *y2T, *hbuf;
    f32x2* stats;
    float* part;
    u64* sync;
    u64* stamps;
};
constexpr int SY_EPOCH = 0, SY_X = 128, SY_K = 512, SY_H = 1024, SY_Y = 1536, SY_P = 2048;

__device__ __forceinline__ void signal(u64* word, u64 tag) { __hip_atomic_store((gu64*)word, tag, __ATOMIC_RELAXED, __HIP_MEMORY_SCOPE_AGENT); }
__device__ __forceinline__ void poll_words(const u64* word, bool want, u64 tag) {
    const u64 t0 = __builtin_amdgcn_s_memrealtime();
    for (;;) {
        const u64 v = want ? __hip_atomic_load((gu64*)word, __ATOMIC_RELAXED, __HIP_MEMORY_SCOPE_AGENT) : tag;
        if (__all(v == tag)) break;
        if (__builtin_amdgcn_s_memrealtime() - t0 > 5000000ull) break;
        __builtin_amdgcn_s_sleep(2);
    }
}
__device__ __forceinline__ bool last_wave(LAS unsigned* cnt, unsigned n) {
    unsigned old = 0; unsigned long long ex; const unsigned addr = (unsigned)(uintptr_t)cnt, one = 1u;
    asm volatile("s_mov_b64 %1, exec\n\ts_mov_b64 exec, 1\n\tds_add_rtn_u32 %0, %2, %3\n\ts_waitcnt lgkmcnt(0)\n\ts_mov_b64 exec, %1" : "+v"(old), "=&s"(ex) : "v"(addr), "v"(one) : "memory");
    return (unsigned)__builtin_amdgcn_readfirstlane(old) == n - 1;
}

constexpr int CV_XB0 = 512, CV_XB1 = 4608 + 512, CV_HE = 9216, CV_HO = 9216 + 4160, CV_WAVE = 20480;
struct ConvPre { u32x4 nh[4]; };
__device__ __forceinline__ void conv_prefetch(const Params& P, int cb, ConvPre& pre) {
    const int wid = __builtin_amdgcn_readfirstlane(threadIdx.x >> 6), lane = threadIdx.x & 63;
#pragma unroll
    for (int i = 0; i < 4; ++i) { const u16* q = P.ssmk + (size_t)(cb * 32 + wid * 4) * SEQ + (i * 64 + lane) * 8; asm volatile("global_load_dwordx4 %0, %1, off nt" : "=&v"(pre.nh[i]) : "v"(q) : "memory"); }
}
__device__ __forceinline__ void conv_block(const Params& P, LAS unsigned char* lds, int cb, ConvPre& pre, int& _sti) {
    const int tid = threadIdx.x, c0 = cb * 32;
    const int wid = __builtin_amdgcn_readfirstlane(tid >> 6), lane = tid & 63;
    LAS unsigned char* wl = lds + wid * CV_WAVE;
    const int chw = c0 + wid * 4;
    const rsrc_t rkv = mk_rsrc(P.kvT, SEQ * DM * 2); const u16* ssmk = P.ssmk;
    u32x4 nx[4], nh[4], no[4];
#define CV_GX(ch) do { _Pragma("unroll") for (int i = 0; i < 4; ++i) nx[i] = __builtin_amdgcn_raw_buffer_load_b128(rkv, (unsigned)(((ch) * SEQ + (i * 64 + lane) * 8) * 2), 0, 16); } while (0)
#define CV_GH(ch) do { _Pragma("unroll") for (int i = 0; i < 4; ++i) nh[i] = ld16_stream(ssmk + (size_t)(ch) * SEQ + (i * 64 + lane) * 8); } while (0)
    CV_GX(chw);
    if (lane < 32) { *(LAS u32x4*)(wl + lane * 16) = (u32x4){0u, 0u, 0u, 0u}; *(LAS u32x4*)(wl + 4608 + lane * 16) = (u32x4){0u, 0u, 0u, 0u}; }
    if (lane < 2) { *(LAS u32x4*)(wl + CV_HE + lane * 16) = (u32x4){0u, 0u, 0u, 0u}; *(LAS u32x4*)(wl + CV_HO + lane * 16) = (u32x4){0u, 0u, 0u, 0u}; }
    const int r = lane & 15, g = lane >> 4, g0 = g & 1, gh = g >> 1;
    const int s0 = r - 8 * g0 + 9 + 16 * gh;
    const int aoff = (r & 1) ? (CV_HE + 2 * s0) : (CV_HO + 2 * (s0 + 1));
    const int boff = 32 * (r - gh) + 16 * g0 - 448;
    const rsrc_t ry = mk_rsrc(P.y2T, SEQ * DM * 2);
    LAS unsigned char* abase[4];
#pragma unroll
    for (int i = 0; i < 4; ++i) { unsigned a = (unsigned)(uintptr_t)(wl + aoff + 1024 * i); asm volatile("" : "+v"(a)); abase[i] = (LAS unsigned char*)(uintptr_t)a; }
    unsigned dmask[4];
#pragma unroll
    for (int i = 0; i < 4; ++i) dmask[i] = (gh == 0 && 8 * g0 + 7 - 2 * i == r) ? 0x0000ffffu : (gh == 0 && 8 * g0 + 6 - 2 * i == r) ? 0xffff0000u : 0u;
    const u32x2 dpair = *(const u32x2*)(P.ssmD + chw);
#define CV_PUTX(par) do { _Pragma("unroll") for (int i = 0; i < 4; ++i) *(LAS u32x4*)(wl + ((par) ? CV_XB1 : CV_XB0) + (i * 64 + lane) * 16) = nx[i]; } while (0)
#define CV_SHIFT() do { unsigned rot[4]; \
        _Pragma("unroll") for (int i = 0; i < 4; ++i) rot[i] = (unsigned)__builtin_amdgcn_update_dpp(0, (int)nh[i][3], 0x13C  , 0xf, 0xf, false); \
        _Pragma("unroll") for (int i = 0; i < 4; ++i) { \
            const unsigned prev = (lane == 0) ? (i == 0 ? 0u : rot[i > 0 ? i - 1 : 0]) : rot[i]; \
            no[i][0] = __builtin_amdgcn_alignbit(nh[i][0], prev, 16); no[i][1] = __builtin_amdgcn_alignbit(nh[i][1], nh[i][0], 16); \
            no[i][2] = __builtin_amdgcn_alignbit(nh[i][2], nh[i][1], 16); no[i][3] = __builtin_amdgcn_alignbit(nh[i][3], nh[i][2], 16); } } while (0)
#define CV_PUTH() do { _Pragma("unroll") for (int i = 0; i < 4; ++i) { \
            *(LAS u32x4*)(wl + CV_HE + 32 + (i * 64 + lane) * 16) = nh[i]; *(LAS u32x4*)(wl + CV_HO + 32 + (i * 64 + lane) * 16) = no[i]; } } while (0)
#define CV_LOAD(buf, par, v) do { _Pragma("unroll") for (int u = 0; u < 8; ++u) { \
            bf[buf][u] = *(const LAS bf16x8*)(wl + ((par) ? CV_XB1 : CV_XB0) + boff + 512 * u + 64 * (7 - (v))); \
            const LAS unsigned* pa = (const LAS unsigned*)(abase[u >> 1] + 64 * (8 * (u & 1) + (v))); \
            u32x4 t; t[0] = pa[0]; t[1] = pa[1]; t[2] = pa[2]; t[3] = pa[3]; \
            af[buf][u] = __builtin_bit_cast(bf16x8, t); } } while (0)
#define CV_EPI(a, ch, c) do { u32x2 o; o[0] = pack_bf16(acc[a][c][0], acc[a][c][1]); o[1] = pack_bf16(acc[a][c][2], acc[a][c][3]); \
            st8(ry, (unsigned)(((ch) * SEQ + 256 * (c) + 16 * r + 4 * g) * 2), o); } while (0)
    bf16x8 af[2][8], bf[2][8];
    f32x4 acc[2][8];
#pragma unroll
    for (int i = 0; i < 4; ++i) nh[i] = pre.nh[i];
    CV_SHIFT(); CV_PUTH();
    CV_PUTX(0);
    CV_LOAD(0, 0, 0);
    CV_GX(chw + 1); CV_GH(chw + 1);
    SCHED();
#pragma unroll
    for (int cc = 0; cc < 4; ++cc) {
        const int a = cc & 1;
        if (cc == 0) __builtin_amdgcn_s_setprio(3); else if (cc == 1) __builtin_amdgcn_s_setprio(2); else if (cc == 2) __builtin_amdgcn_s_setprio(1); else __builtin_amdgcn_s_setprio(0);
        const unsigned dw = dpair[cc >> 1], dd = (cc & 1) ? ((dw & 0xffff0000u) | (dw >> 16)) : ((dw << 16) | (dw & 0xffffu));
        const u32x4 adw = {dd & dmask[0], dd & dmask[1], dd & dmask[2], dd & dmask[3]};
        const bf16x8 ad = __builtin_bit_cast(bf16x8, adw);
#pragma unroll
        for (int v = 0; v < 8; ++v) {
            const int cur = v & 1, nxt = cur ^ 1;
            if (v < 7) CV_LOAD(nxt, a, v + 1);
            else if (cc < 3) { CV_PUTH(); CV_LOAD(nxt, a ^ 1, 0); }
            if (v == 4 && cc < 3) CV_PUTX(a ^ 1);
            if (v == 5 && cc < 3) { CV_SHIFT(); if (cc < 2) CV_GX(chw + cc + 2); }
            if (v == 0 && cc > 0 && cc < 3) CV_GH(chw + cc + 1);
            if (v == 0) {
#pragma unroll
                for (int c = 0; c < 8; ++c) acc[a][c] = __builtin_amdgcn_mfma_f32_16x16x32_bf16(ad, bf[cur][c], (f32x4){0.f, 0.f, 0.f, 0.f}, 0, 0, 0);
            }
#pragma unroll
            for (int u = 0; u < 8; ++u)
#pragma unroll
                for (int c = u; c < 8; ++c) acc[a][c] = __builtin_amdgcn_mfma_f32_16x16x32_bf16(af[cur][u], bf[cur][c - u], acc[a][c], 0, 0, 0);
            if (cc > 0 && v >= 1 && v < 3) {
#pragma unroll
                for (int c = 0; c < 4; ++c) CV_EPI(a ^ 1, chw + cc - 1, 4 * (v - 1) + c);
            }
            if (v == 7) {
                if (cc < 3) {
#pragma unroll
                    for (int i = 0; i < 8; ++i) { __builtin_amdgcn_sched_group_barrier(0x008, 1, 0); __builtin_amdgcn_sched_group_barrier(0x200, 1, 0); }
#pragma unroll
                    for (int i = 0; i < 24; ++i) { __builtin_amdgcn_sched_group_barrier(0x008, 1, 0); __builtin_amdgcn_sched_group_barrier(0x100, 1, 0); }
                }
            } else {
#pragma unroll
                for (int i = 0; i < 24; ++i) { __builtin_amdgcn_sched_group_barrier(0x008, 1, 0); __builtin_amdgcn_sched_group_barrier(0x100, 1, 0); }
                if (v == 0 && cc > 0 && cc < 3) {
#pragma unroll
                    for (int i = 0; i < 4; ++i) { __builtin_amdgcn_sched_group_barrier(0x008, 1, 0); __builtin_amdgcn_sched_group_barrier(0x020, 1, 0); }
                }
                if (cc > 0 && v >= 1 && v < 3) {
#pragma unroll
                    for (int i = 0; i < 4; ++i) { __builtin_amdgcn_sched_group_barrier(0x008, 1, 0); __builtin_amdgcn_sched_group_barrier(0x002, 2, 0); __builtin_amdgcn_sched_group_barrier(0x008, 1, 0); __builtin_amdgcn_sched_group_barrier(0x040, 1, 0); }
                }
                if (v == 4 && cc < 3) {
#pragma unroll
                    for (int i = 0; i < 4; ++i) { __builtin_amdgcn_sched_group_barrier(0x008, 1, 0); __builtin_amdgcn_sched_group_barrier(0x200, 1, 0); }
                }
                if (v == 5 && cc < 3) {
#pragma unroll
                    for (int i = 0; i < 12; ++i) { __builtin_amdgcn_sched_group_barrier(0x008, 1, 0); __builtin_amdgcn_sched_group_barrier(0x002, 2, 0); }
                }
            }
            SCHED();
        }
    }
#pragma unroll
    for (int c = 0; c < 8; ++c) CV_EPI(1, chw + 3, c);
    __builtin_amdgcn_s_setprio(0);
    ST();
#undef CV_GX
#undef CV_GH
#undef CV_PUTX
#undef CV_SHIFT
#undef CV_PUTH
#undef CV_LOAD
#undef CV_EPI
}

constexpr int KB_STAGE = 32768, KC_N1 = 23, LDS_BYTES = 5 * KB_STAGE;
constexpr int SYNC_CNT = 0;
struct G2Load { unsigned vA1, vA2, vB0, vB1; const char *cA, *cB; };
#define KD_B4(pb, pl) do { GLDS((pb) + g2.vB0, (pl) + 16384 + lw * 4096); GLDS((pb) + g2.vB0 + (size_t)4 * DM * 2, (pl) + 16384 + lw * 4096 + 1024); \
        GLDS((pb) + g2.vB1, (pl) + 16384 + lw * 4096 + 2048); GLDS((pb) + g2.vB1 + (size_t)4 * DM * 2, (pl) + 16384 + lw * 4096 + 3072); } while (0)
#define KD_ISSUE1A(s_, slot) do { const char* _a = g2.cA + (size_t)(KC_N1 * ks + (s_)) * 128 + g2.vA1; LAS unsigned char* _l = lds + (slot) * KB_STAGE; \
        GLDS_DEV(_a, _l + lw * 4096); GLDS_DEV(_a + (size_t)8 * FF * 2, _l + lw * 4096 + 1024); GLDS_DEV(_a + (size_t)16 * FF * 2, _l + lw * 4096 + 2048); GLDS_DEV(_a + (size_t)24 * FF * 2, _l + lw * 4096 + 3072); } while (0)
#define KD_ISSUE1B(s_, slot) do { const char* _b = g2.cB + (size_t)(KC_N1 * ks + (s_)) * 64 * DM * 2; KD_B4(_b, lds + (slot) * KB_STAGE); } while (0)
#define KD_ISSUE1(s_, slot) do { KD_ISSUE1A(s_, slot); KD_ISSUE1B(s_, slot); } while (0)
#define KD_ISSUE2(s_, slot) do { const int _kt = KC_N1 + (s_); const char* _a = g2.cA + (size_t)_kt * 128 + g2.vA2; const char* _b = g2.cB + (size_t)_kt * 64 * DM * 2; LAS unsigned char* _l = lds + (slot) * KB_STAGE; \
        GLDS_DEV(_a, _l + ks * 8192 + lw * 2048); GLDS_DEV(_a + (size_t)8 * FF * 2, _l + ks * 8192 + lw * 2048 + 1024); KD_B4(_b, _l); } while (0)
__device__ __forceinline__ G2Load gemm2_addr(const Params& P, int ks, int pm, int pn, int lw, int lane) {
    G2Load g2;
    const unsigned swA = (unsigned)(8 * ((lane & 7) ^ ((lane >> 3) & 7)) * 2);
    g2.vA1 = (unsigned)((lw * 32 + (lane >> 3)) * FF * 2) + swA;
    g2.vA2 = (unsigned)((ks * 64 + lw * 16 + (lane >> 3)) * FF * 2) + swA;
    g2.vB0 = (unsigned)(((lw * 16 + (lane >> 4)) * DM + 8 * ((lane & 15) ^ (((lane >> 4) & 3) << 2))) * 2);
    g2.vB1 = (unsigned)(((lw * 16 + 8 + (lane >> 4)) * DM + 8 * ((lane & 15) ^ ((((lane >> 4) & 3) << 2) | 2))) * 2);
    g2.cA = (const char*)P.hbuf + (size_t)pm * 128 * FF * 2;
    g2.cB = (const char*)P.wd + (size_t)pn * 128 * 2;
    return g2;
}

constexpr int HTB = 16384;
#define SA(b, h) (((b) * 2 + (h)) * HTB)
#define SB(b, h) ((4 + (b) * 2 + (h)) * HTB)
struct G1Addr { unsigned voffA, voffB; const char *cA, *cG, *cU; unsigned ldsw; };
#define STAGE_A(bufoff, h, kt) do { const char* _g = ga.cA + (h) * ((size_t)128 * DM * 2) + (size_t)(kt) * 128; \
        GLDS_DEV(_g + ga.voffA, lds + (bufoff) + ga.ldsw); GLDS_DEV(_g + (size_t)64 * DM * 2 + ga.voffA, lds + (bufoff) + ga.ldsw + 8192); } while (0)
#define STAGE_B(bufoff, base, kt) do { const char* _g = (base) + (size_t)(kt) * ((size_t)64 * FF * 2); \
        GLDS(_g + ga.voffB, lds + (bufoff) + ga.ldsw); GLDS(_g + (size_t)32 * FF * 2 + ga.voffB, lds + (bufoff) + ga.ldsw + 8192); } while (0)
__device__ __forceinline__ G1Addr gemm1_addr(const Params& P, int pm, int pn) {
    const int tid = threadIdx.x, wid = __builtin_amdgcn_readfirstlane(tid >> 6);
    G1Addr ga;
    ga.voffA = (unsigned)(((tid >> 3) * DM + 8 * ((tid & 7) ^ ((tid >> 3) & 7))) * 2);
    const int kb = tid >> 4;
    ga.voffB = (unsigned)((kb * FF + 8 * ((tid & 15) ^ (((kb & 3) << 2) | (((kb >> 3) & 1) << 1)))) * 2);
    ga.cA = (const char*)P.xn + (size_t)pm * 256 * DM * 2;
    ga.cG = (const char*)P.wg + (size_t)pn * 128 * 2;
    ga.cU = (const char*)P.wu + (size_t)pn * 128 * 2;
    ga.ldsw = (unsigned)wid * 1024u;
    return ga;
}
__device__ __forceinline__ void gemm1_block(const Params& P, LAS unsigned char* lds, const G1Addr& ga, const G2Load& g2, int ks, int pm, int pn, int& _sti) {
    const int tid = threadIdx.x, wid = __builtin_amdgcn_readfirstlane(tid >> 6), lane = tid & 63, wr = wid >> 2, wc = wid & 3, fr = lane & 15, fq = lane >> 4;
    constexpr int nt = DM / 64;
    const int q = (lane & 15) >> 2, p = lane & 3;
    const int aoff0 = (wr * 64 + fr) * 128 + 16 * ((fq) ^ (fr & 7)), aoff1 = (wr * 64 + fr) * 128 + 16 * ((4 + fq) ^ (fr & 7));
    const int boff0 = 256 * (8 * fq + q) + 16 * (4 * (wc ^ q) + 2 * (0 ^ (fq & 1)) + (p >> 1)) + 8 * (p & 1);
    const int boff1 = 256 * (8 * fq + q) + 16 * (4 * (wc ^ q) + 2 * (1 ^ (fq & 1)) + (p >> 1)) + 8 * (p & 1);
#define LDA(dst, b, h) do { _Pragma("unroll") for (int m = 0; m < 4; ++m) { \
        dst[m][0] = *(const LAS bf16x8*)(lds + SA(b, h) + aoff0 + m * 2048); dst[m][1] = *(const LAS bf16x8*)(lds + SA(b, h) + aoff1 + m * 2048); } } while (0)
#define LDB(dst, b, h) do { \
        TRRD(dst[0][0][0], bb0, SB(b, h) - SB(0, 0)); TRRD(dst[0][0][1], bb0, SB(b, h) - SB(0, 0) + 1024); TRRD(dst[1][0][0], bb1, SB(b, h) - SB(0, 0)); TRRD(dst[1][0][1], bb1, SB(b, h) - SB(0, 0) + 1024); \
        TRRD(dst[0][1][0], bb0, SB(b, h) - SB(0, 0) + 8192); TRRD(dst[0][1][1], bb0, SB(b, h) - SB(0, 0) + 9216); TRRD(dst[1][1][0], bb1, SB(b, h) - SB(0, 0) + 8192); TRRD(dst[1][1][1], bb1, SB(b, h) - SB(0, 0) + 9216); } while (0)
#define WAIT_LB(dst) asm volatile("s_waitcnt lgkmcnt(0)" : "+v"(dst[0][0][0]), "+v"(dst[0][0][1]), "+v"(dst[0][1][0]), "+v"(dst[0][1][1]), "+v"(dst[1][0][0]), "+v"(dst[1][0][1]), "+v"(dst[1][1][0]), "+v"(dst[1][1][1]) :: "memory")
#define MMA(ai, bj, At, Bt) do { __builtin_amdgcn_s_setprio(1); _Pragma("unroll") for (int m = 0; m < 4; ++m) _Pragma("unroll") for (int n = 0; n < 2; ++n) _Pragma("unroll") for (int k = 0; k < 2; ++k) \
        acc[ai][bj][m][n] = __builtin_amdgcn_mfma_f32_16x16x32_bf16(CAT(Bt[n][k][0], Bt[n][k][1]), At[m][k], acc[ai][bj][m][n], 0, 0, 0); __builtin_amdgcn_s_setprio(0); } while (0)
    f32x4 acc[2][2][4][2];
#pragma unroll
    for (int a = 0; a < 2; ++a)
#pragma unroll
        for (int b = 0; b < 2; ++b)
#pragma unroll
            for (int m = 0; m < 4; ++m)
#pragma unroll
                for (int n = 0; n < 2; ++n) acc[a][b][m][n] = (f32x4){0.f, 0.f, 0.f, 0.f};
    bf16x8 At[4][2]; bf16x4 B0[2][2][2], B1[2][2][2];
    const unsigned bb0 = (unsigned)(uintptr_t)(lds + SB(0, 0) + boff0), bb1 = (unsigned)(uintptr_t)(lds + SB(0, 0) + boff1);
    if (wr == 1) BAR();
    WAIT_V(2); WAIT_L(0); BAR(); BAR();
    ST();
    for (int t = 0; t < nt - 2; t += 2) {
        LDB(B0, 0, 0); SCHED(); LDA(At, 0, 0); STAGE_A(SA(1, 1), 1, t + 1);
        WAIT_L(8); BAR(); WAIT_LB(B0); SCHED(); MMA(0, 0, At, B0); BAR(); SCHED();
        LDB(B1, 0, 1); STAGE_B(SB(0, 0), ga.cG, t + 2);
        BAR(); WAIT_LB(B1); SCHED(); MMA(0, 1, At, B1); BAR();
        LDA(At, 0, 1); STAGE_A(SA(0, 0), 0, t + 2);
        BAR(); WAIT_L(0); MMA(1, 0, At, B0); BAR(); SCHED();
        STAGE_B(SB(0, 1), ga.cU, t + 2);
        WAIT_V(6); BAR(); MMA(1, 1, At, B1); BAR();
        LDB(B0, 1, 0); SCHED(); LDA(At, 1, 0); STAGE_A(SA(0, 1), 1, t + 2);
        WAIT_L(8); BAR(); WAIT_LB(B0); SCHED(); MMA(0, 0, At, B0); BAR(); SCHED();
        LDB(B1, 1, 1); STAGE_B(SB(1, 0), ga.cG, t + 3);
        BAR(); WAIT_LB(B1); SCHED(); MMA(0, 1, At, B1); BAR();
        LDA(At, 1, 1); STAGE_A(SA(1, 0), 0, t + 3);
        BAR(); WAIT_L(0); MMA(1, 0, At, B0); BAR(); SCHED();
        STAGE_B(SB(1, 1), ga.cU, t + 3);
        WAIT_V(6); BAR(); MMA(1, 1, At, B1); BAR();
    }
    {
        LDB(B0, 0, 0); SCHED(); LDA(At, 0, 0); STAGE_A(SA(1, 1), 1, nt - 1);
        WAIT_L(8); BAR(); WAIT_LB(B0); SCHED(); MMA(0, 0, At, B0); BAR(); SCHED();
        LDB(B1, 0, 1); BAR(); WAIT_LB(B1); SCHED(); MMA(0, 1, At, B1); BAR();
        LDA(At, 0, 1); BAR(); WAIT_L(0); MMA(1, 0, At, B0); BAR(); SCHED();
        WAIT_V(0); BAR(); MMA(1, 1, At, B1); BAR();
    }
    {
        LDB(B0, 1, 0); SCHED(); LDA(At, 1, 0); BAR(); WAIT_LB(B0); SCHED(); MMA(0, 0, At, B0); BAR(); SCHED();
        LDB(B1, 1, 1); BAR(); WAIT_LB(B1); SCHED(); MMA(0, 1, At, B1); BAR();
        LDA(At, 1, 1); BAR(); WAIT_L(0); MMA(1, 0, At, B0); MMA(1, 1, At, B1);
    }
    if (tid == 0) *(LAS unsigned*)(lds + SYNC_CNT) = 0u;
    if (wr == 0) BAR();
    WAIT_L(0); BAR();
    ST();
    if (wid >= 4) { const int lw = wid - 4; KD_ISSUE1B(0, 0); KD_ISSUE1B(1, 1); KD_ISSUE1B(2, 2); KD_ISSUE1B(3, 3); }
    int tz = threadIdx.x; asm volatile("" : "+v"(tz));
    const int el = tz & 63, efr = el & 15, efq = el >> 4;
    {
        const int gb = (blockIdx.x & 7) * 28 + (blockIdx.x >> 3);
        if (wid < 4) __builtin_amdgcn_global_load_lds((const GAS unsigned*)((const char*)P.wd + ((size_t)(gb * 4 + wid) * 64 + el) * 128), (LAS unsigned*)(lds + 2 * KB_STAGE + wid * 256), 4, 0, 0);
    }
    const rsrc_t rh = mk_rsrc(P.hbuf, SEQ * FF * 2);
    const unsigned hoff = (unsigned)(((pm * 256 + wr * 64 + efr) * FF + pn * 128 + wc * 32 + 16 * (efq & 1) + 8 * (efq >> 1)) * 2);
#pragma unroll
    for (int ai = 0; ai < 2; ++ai)
#pragma unroll
        for (int m = 0; m < 4; ++m) {
            unsigned pk[2][2];
#pragma unroll
            for (int n = 0; n < 2; ++n) {
                float hv[4];
#pragma unroll
                for (int j = 0; j < 4; ++j) {
                    const float e = __builtin_amdgcn_exp2f(acc[ai][0][m][n][j] * -1.4426950408889634f);
                    hv[j] = acc[ai][1][m][n][j] * __builtin_amdgcn_rcpf(1.0f + e);
                }
                pk[n][0] = pack_bf16(hv[0], hv[1]); pk[n][1] = pack_bf16(hv[2], hv[3]);
            }
            const auto s0 = __builtin_amdgcn_permlane16_swap(pk[0][0], pk[1][0], false, false);
            const auto s1 = __builtin_amdgcn_permlane16_swap(pk[0][1], pk[1][1], false, false);
            st16(rh, hoff + (unsigned)((ai * 128 + m * 16) * FF * 2), (u32x4){s0[0], s1[0], s0[1], s1[1]});
        }
    ST();
#undef LDA
#undef LDB
#undef WAIT_LB
#undef MMA
}

#define KE_S01(XA, XB, YA, YB, ya0, ya1, yb0, yb1, yb2, yb3) do { \
        TRRD(YB[0][0][0], yb0, 0); TRRD(YB[0][0][1], yb0, 1024); RD128(YA[0][0], ya0, 0); TRRD(YB[0][1][0], yb1, 0); TRRD(YB[0][1][1], yb1, 1024); RD128(YA[0][1], ya0, 2048); TRRD(YB[0][2][0], yb2, 0); TRRD(YB[0][2][1], yb2, 1024); RD128(YA[0][2], ya0, 4096); TRRD(YB[0][3][0], yb3, 0); TRRD(YB[0][3][1], yb3, 1024); RD128(YA[0][3], ya0, 6144); TRRD(YB[1][0][0], yb0, 8192); TRRD(YB[1][0][1], yb0, 9216); RD128(YA[1][0], ya1, 0); TRRD(YB[1][1][0], yb1, 8192); TRRD(YB[1][1][1], yb1, 9216); RD128(YA[1][1], ya1, 2048); TRRD(YB[1][2][0], yb2, 8192); TRRD(YB[1][2][1], yb2, 9216); RD128(YA[1][2], ya1, 4096); TRRD(YB[1][3][0], yb3, 8192); TRRD(YB[1][3][1], yb3, 9216); RD128(YA[1][3], ya1, 6144); SCHED(); } while (0)
#define KE_S11(XA, XB, YA, YB, ya0, ya1, yb0, yb1, yb2, yb3) do { \
        KE_M(XA, XB, 0, 0, 0); SCHED(); TRRD(YB[0][0][0], yb0, 0); SCHED(); \
        KE_M(XA, XB, 0, 0, 1); SCHED(); TRRD(YB[0][0][1], yb0, 1024); SCHED(); \
        KE_M(XA, XB, 0, 0, 2); SCHED(); RD128(YA[0][0], ya0, 0); SCHED(); \
        KE_M(XA, XB, 0, 0, 3); SCHED(); TRRD(YB[0][1][0], yb1, 0); SCHED(); \
        KE_M(XA, XB, 0, 1, 0); SCHED(); TRRD(YB[0][1][1], yb1, 1024); SCHED(); \
        KE_M(XA, XB, 0, 1, 1); SCHED(); RD128(YA[0][1], ya0, 2048); SCHED(); \
        KE_M(XA, XB, 0, 1, 2); SCHED(); TRRD(YB[0][2][0], yb2, 0); SCHED(); \
        KE_M(XA, XB, 0, 1, 3); SCHED(); TRRD(YB[0][2][1], yb2, 1024); SCHED(); \
        KE_M(XA, XB, 0, 2, 0); SCHED(); RD128(YA[0][2], ya0, 4096); SCHED(); \
        KE_M(XA, XB, 0, 2, 1); SCHED(); TRRD(YB[0][3][0], yb3, 0); SCHED(); \
        KE_M(XA, XB, 0, 2, 2); SCHED(); TRRD(YB[0][3][1], yb3, 1024); SCHED(); \
        KE_M(XA, XB, 0, 2, 3); SCHED(); RD128(YA[0][3], ya0, 6144); SCHED(); \
        KE_M(XA, XB, 0, 3, 0); SCHED(); TRRD(YB[1][0][0], yb0, 8192); SCHED(); \
        KE_M(XA, XB, 0, 3, 1); SCHED(); TRRD(YB[1][0][1], yb0, 9216); SCHED(); \
        KE_M(XA, XB, 0, 3, 2); SCHED(); RD128(YA[1][0], ya1, 0); SCHED(); \
        KE_M(XA, XB, 0, 3, 3); SCHED(); TRRD(YB[1][1][0], yb1, 8192); SCHED(); \
        KE_M(XA, XB, 1, 0, 0); SCHED(); TRRD(YB[1][1][1], yb1, 9216); SCHED(); \
        KE_M(XA, XB, 1, 0, 1); SCHED(); RD128(YA[1][1], ya1, 2048); SCHED(); \
        KE_M(XA, XB, 1, 0, 2); SCHED(); TRRD(YB[1][2][0], yb2, 8192); SCHED(); \
        KE_M(XA, XB, 1, 0, 3); SCHED(); TRRD(YB[1][2][1], yb2, 9216); SCHED(); \
        KE_M(XA, XB, 1, 1, 0); SCHED(); RD128(YA[1][2], ya1, 4096); SCHED(); \
        KE_M(XA, XB, 1, 1, 1); SCHED(); TRRD(YB[1][3][0], yb3, 8192); SCHED(); \
        KE_M(XA, XB, 1, 1, 2); SCHED(); TRRD(YB[1][3][1], yb3, 9216); SCHED(); \
        KE_M(XA, XB, 1, 1, 3); SCHED(); RD128(YA[1][3], ya1, 6144); SCHED(); \
        KE_M(XA, XB, 1, 2, 0); SCHED(); SCHED(); \
        KE_M(XA, XB, 1, 2, 1); SCHED(); SCHED(); \
        KE_M(XA, XB, 1, 2, 2); SCHED(); SCHED(); \
        KE_M(XA, XB, 1, 2, 3); SCHED(); SCHED(); \
        KE_M(XA, XB, 1, 3, 0); SCHED(); SCHED(); \
        KE_M(XA, XB, 1, 3, 1); SCHED(); SCHED(); \
        KE_M(XA, XB, 1, 3, 2); SCHED(); SCHED(); \
        KE_M(XA, XB, 1, 3, 3); SCHED(); SCHED(); } while (0)
#define KE_S12(XA, XB, YA, YB, ya0, ya1, yb0, yb1, yb2, yb3) do { \
        KE_M(XA, XB, 0, 0, 0); SCHED(); TRRD(YB[0][0][0], yb0, 0); SCHED(); \
        KE_M(XA, XB, 0, 0, 1); SCHED(); TRRD(YB[0][0][1], yb0, 1024); SCHED(); \
        KE_M(XA, XB, 0, 0, 2); SCHED(); RD128(YA[0][0], ya0, 0); SCHED(); \
        KE_M(XA, XB, 0, 0, 3); SCHED(); TRRD(YB[0][1][0], yb1, 0); SCHED(); \
        KE_M(XA, XB, 0, 1, 0); SCHED(); TRRD(YB[0][1][1], yb1, 1024); SCHED(); \
        KE_M(XA, XB, 0, 1, 1); SCHED(); RD128(YA[0][1], ya0, 2048); SCHED(); \
        KE_M(XA, XB, 0, 1, 2); SCHED(); TRRD(YB[0][2][0], yb2, 0); SCHED(); \
        KE_M(XA, XB, 0, 1, 3); SCHED(); TRRD(YB[0][2][1], yb2, 1024); SCHED(); \
        KE_M(XA, XB, 0, 2, 0); SCHED(); TRRD(YB[0][3][0], yb3, 0); SCHED(); \
        KE_M(XA, XB, 0, 2, 1); SCHED(); TRRD(YB[0][3][1], yb3, 1024); SCHED(); \
        KE_M(XA, XB, 0, 2, 2); SCHED(); TRRD(YB[1][0][0], yb0, 8192); SCHED(); \
        KE_M(XA, XB, 0, 2, 3); SCHED(); TRRD(YB[1][0][1], yb0, 9216); SCHED(); \
        KE_M(XA, XB, 0, 3, 0); SCHED(); RD128(YA[1][0], ya1, 0); SCHED(); \
        KE_M(XA, XB, 0, 3, 1); SCHED(); TRRD(YB[1][1][0], yb1, 8192); SCHED(); \
        KE_M(XA, XB, 0, 3, 2); SCHED(); TRRD(YB[1][1][1], yb1, 9216); SCHED(); \
        KE_M(XA, XB, 0, 3, 3); SCHED(); RD128(YA[1][1], ya1, 2048); SCHED(); \
        KE_M(XA, XB, 1, 0, 0); SCHED(); TRRD(YB[1][2][0], yb2, 8192); SCHED(); \
        KE_M(XA, XB, 1, 0, 1); SCHED(); TRRD(YB[1][2][1], yb2, 9216); SCHED(); \
        KE_M(XA, XB, 1, 0, 2); SCHED(); TRRD(YB[1][3][0], yb3, 8192); SCHED(); \
        KE_M(XA, XB, 1, 0, 3); SCHED(); TRRD(YB[1][3][1], yb3, 9216); SCHED(); \
        KE_M(XA, XB, 1, 1, 0); SCHED(); SCHED(); \
        KE_M(XA, XB, 1, 1, 1); SCHED(); SCHED(); \
        KE_M(XA, XB, 1, 1, 2); SCHED(); SCHED(); \
        KE_M(XA, XB, 1, 1, 3); SCHED(); SCHED(); \
        KE_M(XA, XB, 1, 2, 0); SCHED(); SCHED(); \
        KE_M(XA, XB, 1, 2, 1); SCHED(); SCHED(); \
        KE_M(XA, XB, 1, 2, 2); SCHED(); SCHED(); \
        KE_M(XA, XB, 1, 2, 3); SCHED(); SCHED(); \
        KE_M(XA, XB, 1, 3, 0); SCHED(); SCHED(); \
        KE_M(XA, XB, 1, 3, 1); SCHED(); SCHED(); \
        KE_M(XA, XB, 1, 3, 2); SCHED(); SCHED(); \
        KE_M(XA, XB, 1, 3, 3); SCHED(); SCHED(); } while (0)
#define KE_S22(XA, XB, YA, YB, ya0, ya1, yb0, yb1, yb2, yb3) do { \
        KE_M(XA, XB, 0, 0, 0); SCHED(); TRRD(YB[0][0][0], yb0, 0); TRRD(YB[0][0][1], yb0, 1024); SCHED(); \
        KE_M(XA, XB, 0, 0, 1); SCHED(); RD128(YA[0][0], ya0, 0); TRRD(YB[0][1][0], yb1, 0); SCHED(); \
        KE_M(XA, XB, 0, 0, 2); SCHED(); TRRD(YB[0][1][1], yb1, 1024); RD128(YA[0][1], ya0, 2048); SCHED(); \
        KE_M(XA, XB, 0, 0, 3); SCHED(); TRRD(YB[0][2][0], yb2, 0); TRRD(YB[0][2][1], yb2, 1024); SCHED(); \
        KE_M(XA, XB, 0, 1, 0); SCHED(); TRRD(YB[0][3][0], yb3, 0); SCHED(); \
        KE_M(XA, XB, 0, 1, 1); SCHED(); TRRD(YB[0][3][1], yb3, 1024); SCHED(); \
        KE_M(XA, XB, 0, 1, 2); SCHED(); TRRD(YB[1][0][0], yb0, 8192); SCHED(); \
        KE_M(XA, XB, 0, 1, 3); SCHED(); TRRD(YB[1][0][1], yb0, 9216); SCHED(); \
        KE_M(XA, XB, 1, 0, 0); SCHED(); RD128(YA[1][0], ya1, 0); SCHED(); \
        KE_M(XA, XB, 1, 0, 1); SCHED(); TRRD(YB[1][1][0], yb1, 8192); SCHED(); \
        KE_M(XA, XB, 1, 0, 2); SCHED(); TRRD(YB[1][1][1], yb1, 9216); SCHED(); \
        KE_M(XA, XB, 1, 0, 3); SCHED(); RD128(YA[1][1], ya1, 2048); SCHED(); \
        KE_M(XA, XB, 1, 1, 0); SCHED(); TRRD(YB[1][2][0], yb2, 8192); SCHED(); \
        KE_M(XA, XB, 1, 1, 1); SCHED(); TRRD(YB[1][2][1], yb2, 9216); SCHED(); \
        KE_M(XA, XB, 1, 1, 2); SCHED(); TRRD(YB[1][3][0], yb3, 8192); SCHED(); \
        KE_M(XA, XB, 1, 1, 3); SCHED(); TRRD(YB[1][3][1], yb3, 9216); SCHED(); } while (0)
#define KE_S20(XA, XB, YA, YB, ya0, ya1, yb0, yb1, yb2, yb3) do { \
        KE_M(XA, XB, 0, 0, 0); SCHED(); SCHED(); \
        KE_M(XA, XB, 0, 0, 1); SCHED(); SCHED(); \
        KE_M(XA, XB, 0, 0, 2); SCHED(); SCHED(); \
        KE_M(XA, XB, 0, 0, 3); SCHED(); SCHED(); \
        KE_M(XA, XB, 0, 1, 0); SCHED(); SCHED(); \
        KE_M(XA, XB, 0, 1, 1); SCHED(); SCHED(); \
        KE_M(XA, XB, 0, 1, 2); SCHED(); SCHED(); \
        KE_M(XA, XB, 0, 1, 3); SCHED(); SCHED(); \
        KE_M(XA, XB, 1, 0, 0); SCHED(); SCHED(); \
        KE_M(XA, XB, 1, 0, 1); SCHED(); SCHED(); \
        KE_M(XA, XB, 1, 0, 2); SCHED(); SCHED(); \
        KE_M(XA, XB, 1, 0, 3); SCHED(); SCHED(); \
        KE_M(XA, XB, 1, 1, 0); SCHED(); SCHED(); \
        KE_M(XA, XB, 1, 1, 1); SCHED(); SCHED(); \
        KE_M(XA, XB, 1, 1, 2); SCHED(); SCHED(); \
        KE_M(XA, XB, 1, 1, 3); SCHED(); SCHED(); } while (0)

__device__ __forceinline__ void gemm2_loader(const Params& P, LAS unsigned char* lds, const G2Load& g2, int ks, int pm, int pn, int lw, int lane) {
    const int spare = (1 - ks) * 8192;
    u32x4 ey[4], ex[4]; unsigned es;
#define KD_LDY(i) do { const int row = (lw * 4 + (i)) * 8 + (lane >> 3), ck = (lane & 7) ^ (((row >> 1) & 3) << 1); \
        const char* _p = (const char*)P.y2T + ((size_t)(pn * 128 + row) * SEQ + pm * 128 + ks * 64 + 8 * ck) * 2; \
        asm volatile("global_load_dwordx4 %0, %1, off sc1" : "=&v"(ey[i]) : "v"(_p) : "memory"); } while (0)
#define KD_LDX(i) do { const int xr = (lw * 4 + (i)) * 4 + (lane >> 4), xc = (lane & 15) ^ (xr & 15); \
        const char* _p = (const char*)P.x + ((size_t)(pm * 128 + ks * 64 + xr) * DM + pn * 128 + 8 * xc) * 2; \
        asm volatile("global_load_dwordx4 %0, %1, off" : "=&v"(ex[i]) : "v"(_p) : "memory"); } while (0)
#define KD_LDS() do { const char* _p = (const char*)P.stats + (size_t)pm * 128 * 8 + (lw * 64 + lane) * 4; \
        asm volatile("global_load_dword %0, %1, off sc1" : "=&v"(es) : "v"(_p) : "memory"); } while (0)
#define KD_PUT() do { asm volatile("" : "+v"(ey[0]), "+v"(ey[1]), "+v"(ey[2]), "+v"(ey[3]), "+v"(ex[0]), "+v"(ex[1]), "+v"(ex[2]), "+v"(ex[3]), "+v"(es)); \
        _Pragma("unroll") for (int i = 0; i < 4; ++i) { \
            *(LAS u32x4*)(lds + (2 + (lw >> 1)) * KB_STAGE + spare + ((lw & 1) * 4 + i) * 1024 + lane * 16) = ey[i]; \
            *(LAS u32x4*)(lds + (lw >> 1) * KB_STAGE + spare + ((lw & 1) * 4 + i) * 1024 + lane * 16) = ex[i]; } \
        *(LAS unsigned*)(lds + 4 * KB_STAGE + spare + 1024 + (lw * 64 + lane) * 4) = es; } while (0)
        KD_ISSUE1A(0, 0); KD_ISSUE1A(1, 1); KD_ISSUE1A(2, 2); KD_ISSUE1A(3, 3);
        WAIT_V(12); BAR();
        WAIT_V(8); BAR(); KD_ISSUE1(4, 4);
        WAIT_V(12); BAR(); KD_ISSUE1(5, 0);
        WAIT_V(16); BAR(); KD_LDY(0); KD_ISSUE1(6, 1);
        WAIT_V(17); BAR(); KD_ISSUE1(7, 2);
        WAIT_V(17); BAR(); KD_LDX(0); KD_ISSUE1(8, 3);
        WAIT_V(17); BAR(); KD_ISSUE1(9, 4);
        WAIT_V(17); BAR(); KD_LDY(1); KD_ISSUE1(10, 0);
        WAIT_V(17); BAR(); KD_ISSUE1(11, 1);
        WAIT_V(17); BAR(); KD_LDX(1); KD_ISSUE1(12, 2);
        WAIT_V(17); BAR(); KD_ISSUE1(13, 3);
        WAIT_V(17); BAR(); KD_LDY(2); KD_ISSUE1(14, 4);
        WAIT_V(17); BAR(); KD_ISSUE1(15, 0);
        WAIT_V(17); BAR(); KD_LDX(2); KD_ISSUE1(16, 1);
        WAIT_V(17); BAR(); KD_ISSUE1(17, 2);
        WAIT_V(17); BAR(); KD_LDY(3); KD_ISSUE1(18, 3);
        WAIT_V(17); BAR(); KD_ISSUE1(19, 4);
        WAIT_V(17); BAR(); KD_LDX(3); KD_ISSUE1(20, 0);
        WAIT_V(17); BAR(); KD_ISSUE1(21, 1);
        WAIT_V(17); BAR(); KD_LDS(); KD_ISSUE1(22, 2);
        WAIT_V(17); BAR(); KD_ISSUE2(23, 3);
        WAIT_V(15); BAR(); KD_ISSUE2(24, 4);
        WAIT_V(12); BAR(); KD_ISSUE2(25, 0);
        WAIT_V(12); BAR(); KD_ISSUE2(26, 1); BAR();
        WAIT_V(12); BAR(); KD_PUT(); KD_ISSUE2(27, 2); WAIT_L(0);
        WAIT_V(12); BAR(); KD_ISSUE2(28, 3);
        WAIT_V(12); BAR(); KD_ISSUE2(29, 4);
        WAIT_V(12); BAR(); KD_ISSUE2(30, 0);
        WAIT_V(12); BAR(); KD_ISSUE2(31, 1);
        WAIT_V(12); BAR(); KD_ISSUE2(32, 2);
        WAIT_V(12); BAR();
        WAIT_V(6); BAR();
        WAIT_V(0); BAR();
#undef KD_LDY
#undef KD_LDX
#undef KD_LDS
#undef KD_PUT
}
__device__ __forceinline__ void gemm2_mult(const Params& P, LAS unsigned char* lds, int ks, int pm, int pn, int wid, int lane, u64 tag, int& _sti) {
    const int tid = threadIdx.x, spare = (1 - ks) * 8192;
    const int wr = wid >> 1, wc = wid & 1, rg = (wr != ks) ? 1 : 0, fr = lane & 15, fq = lane >> 4, q = (lane & 15) >> 2, p = lane & 3;
    const unsigned ldsb = (unsigned)(uintptr_t)lds;
    const int a1row = wr * 64 + fr, a2row = ks * 64 + rg * 32 + fr;
    const unsigned a1k0 = ldsb + a1row * 128 + 16 * ((0 + fq) ^ (fr & 7)), a1k1 = ldsb + a1row * 128 + 16 * ((4 + fq) ^ (fr & 7));
    const unsigned a2k0 = ldsb + a2row * 128 + 16 * ((0 + fq) ^ (fr & 7)), a2k1 = ldsb + a2row * 128 + 16 * ((4 + fq) ^ (fr & 7));
    unsigned bo[4];
#pragma unroll
    for (int n = 0; n < 4; ++n) bo[n] = ldsb + 16384 + 256 * (8 * fq + q) + 16 * ((8 * wc + 2 * n + (p >> 1)) ^ (4 * q + 2 * (fq & 1))) + 8 * (p & 1);
#define KE_M(Af, Bf, kk, m, n) acc[m][n] = __builtin_amdgcn_mfma_f32_16x16x32_bf16(CAT(Bf[kk][n][0], Bf[kk][n][1]), Af[kk][m], acc[m][n], 0, 0, 0)
#define KE_WAITL(Af, Bf, NM) do { asm volatile("s_waitcnt lgkmcnt(0)" : "+v"(Bf[0][0][0]), "+v"(Bf[0][0][1]), "+v"(Bf[0][1][0]), "+v"(Bf[0][1][1]), "+v"(Bf[0][2][0]), "+v"(Bf[0][2][1]), "+v"(Bf[0][3][0]), "+v"(Bf[0][3][1]), \
        "+v"(Bf[1][0][0]), "+v"(Bf[1][0][1]), "+v"(Bf[1][1][0]), "+v"(Bf[1][1][1]), "+v"(Bf[1][2][0]), "+v"(Bf[1][2][1]), "+v"(Bf[1][3][0]), "+v"(Bf[1][3][1]) :: "memory"); \
        asm volatile("" : "+v"(Af[0][0]), "+v"(Af[0][1]), "+v"(Af[1][0]), "+v"(Af[1][1])); if ((NM) == 4) asm volatile("" : "+v"(Af[0][2]), "+v"(Af[0][3]), "+v"(Af[1][2]), "+v"(Af[1][3])); } while (0)
    f32x4 acc[4][4];
#pragma unroll
    for (int m = 0; m < 4; ++m)
#pragma unroll
        for (int n = 0; n < 4; ++n) acc[m][n] = (f32x4){0.f, 0.f, 0.f, 0.f};
    bf16x8 PA[2][4], QA[2][4]; bf16x4 PB[2][4][2], QB[2][4][2];
    int so = 0;
#define KD_ADDR(AK0, AK1) const unsigned _a0 = (AK0) + so, _a1 = (AK1) + so, _b0 = bo[0] + so, _b1 = bo[1] + so, _b2 = bo[2] + so, _b3 = bo[3] + so
#define KD_NEXT() so = (so == 4 * KB_STAGE) ? 0 : so + KB_STAGE
    BAR();
    ST();
    { KD_ADDR(a1k0, a1k1); KE_S01(QA, QB, PA, PB, _a0, _a1, _b0, _b1, _b2, _b3); KD_NEXT(); }
#define KE_STEP(MAC, NM, AK0, AK1, XA, XB, YA, YB) do { KD_ADDR(AK0, AK1); unsigned _u0 = _a0, _u1 = _a1, _v0 = _b0, _v1 = _b1, _v2 = _b2, _v3 = _b3; \
        asm volatile("" : "+v"(_u0), "+v"(_u1), "+v"(_v0), "+v"(_v1), "+v"(_v2), "+v"(_v3)); \
        KE_WAITL(XA, XB, NM); BAR(); SCHED(); MAC(XA, XB, YA, YB, _u0, _u1, _v0, _v1, _v2, _v3); KD_NEXT(); } while (0)
    const int erow = pm * 128 + ks * 64 + rg * 32 + fr;
    const int ecol = pn * 128 + wc * 64 + 16 * (fq & 1) + 8 * (fq >> 1);
    for (int s = 0; s < 22; s += 2) {
        KE_STEP(KE_S11, 4, a1k0, a1k1, PA, PB, QA, QB);
        KE_STEP(KE_S11, 4, a1k0, a1k1, QA, QB, PA, PB);
    }
    ST();
    KE_STEP(KE_S12, 4, a2k0, a2k1, PA, PB, QA, QB);
    const rsrc_t rp = mk_rsrc(P.part, 256 * 16384);
    LAS unsigned* cnt = (LAS unsigned*)(lds + 4 * KB_STAGE + spare);
    {
        LAS f32x4* ho = (LAS f32x4*)(lds + wc * KB_STAGE + spare);
        if (rg == 0) {
#pragma unroll
            for (int m = 0; m < 2; ++m)
#pragma unroll
                for (int n = 0; n < 4; ++n) ho[(m * 4 + n) * 64 + lane] = acc[2 + m][n];
            if (tid == (ks << 7)) *cnt = 0u;
        } else {
#pragma unroll
            for (int m = 0; m < 4; ++m)
#pragma unroll
                for (int h = 0; h < 2; ++h) {
                    const f32x4 a0 = acc[m][2 * h], a1 = acc[m][2 * h + 1];
                    st16(rp, blockIdx.x * 16384u + (unsigned)(wc * 8192 + (m * 2 + h) * 1024 + lane * 16),
                         (u32x4){pack_bf16(a0[0], a0[1]), pack_bf16(a0[2], a0[3]), pack_bf16(a1[0], a1[1]), pack_bf16(a1[2], a1[3])});
                }
        }
        WAIT_L(0); BAR();
        if (rg == 1) {
#pragma unroll
            for (int m = 0; m < 2; ++m)
#pragma unroll
                for (int n = 0; n < 4; ++n) acc[m][n] = ho[(m * 4 + n) * 64 + lane];
        }
    }
    KE_STEP(KE_S22, 2, a2k0, a2k1, QA, QB, PA, PB);
    KE_STEP(KE_S22, 2, a2k0, a2k1, PA, PB, QA, QB);
    KE_STEP(KE_S22, 2, a2k0, a2k1, QA, QB, PA, PB);
    KE_STEP(KE_S22, 2, a2k0, a2k1, PA, PB, QA, QB);
    if (rg == 1) {
        WAIT_V(0);
        if (lane == 0) {
            const unsigned n = __hip_atomic_fetch_add(cnt, 1u, __ATOMIC_RELAXED, __HIP_MEMORY_SCOPE_WORKGROUP);
            if (n == 1u) signal(P.sync + SY_P + blockIdx.x * 16, tag);
        }
    }
    KE_STEP(KE_S22, 2, a2k0, a2k1, QA, QB, PA, PB);
    KE_STEP(KE_S22, 2, a2k0, a2k1, PA, PB, QA, QB);
    KE_STEP(KE_S22, 2, a2k0, a2k1, QA, QB, PA, PB);
    u64 pf;
    {
        const u64* pfl = P.sync + SY_P + (blockIdx.x ^ 1) * 16;
        asm volatile("global_load_dwordx2 %0, %1, off sc1" : "=&v"(pf) : "v"(pfl) : "memory");
    }
    KE_STEP(KE_S22, 2, a2k0, a2k1, PA, PB, QA, QB);
    KE_STEP(KE_S22, 2, a2k0, a2k1, QA, QB, PA, PB);
    ST();
    asm volatile("s_waitcnt vmcnt(0)" : "+v"(pf) :: "memory");
    if (!__all(pf == tag)) poll_words(P.sync + SY_P + (blockIdx.x ^ 1) * 16, true, tag);
    asm volatile("" ::: "memory");
    u32x4 pin[2][2];
#pragma unroll
    for (int m = 0; m < 2; ++m)
#pragma unroll
        for (int h = 0; h < 2; ++h) {
            const unsigned o = (blockIdx.x ^ 1) * 16384u + (unsigned)(wc * 8192 + ((2 * rg + m) * 2 + h) * 1024 + lane * 16);
            asm volatile("buffer_load_dwordx4 %0, %1, %2, 0 offen sc1" : "=&v"(pin[m][h]) : "v"(o), "s"(rp) : "memory");
        }
    KE_WAITL(PA, PB, 2); SCHED();
    KE_S20(PA, PB, QA, QB, 0, 0, 0, 0, 0, 0);
    ST();
    bf16x4 yv[2][2][2]; u32x4 xv[2][2]; f32x2 st[2];
#pragma unroll
    for (int m = 0; m < 2; ++m) {
        st[m] = *(const LAS f32x2*)(lds + 4 * KB_STAGE + spare + 1024 + (ks * 64 + rg * 32 + 16 * m + fr) * 8);
#pragma unroll
        for (int h = 0; h < 2; ++h) {
            const int xr = 16 * m + fr, xc = 8 * wc + 4 * h + 2 * (fq & 1) + (fq >> 1);
            xv[m][h] = *(const LAS u32x4*)(lds + rg * KB_STAGE + spare + xr * 256 + 16 * (xc ^ (xr & 15)));
        }
    }
#pragma unroll
    for (int m = 0; m < 2; ++m)
#pragma unroll
        for (int h = 0; h < 2; ++h) {
            const int crow = 32 * h + 16 * (fq & 1) + 8 * (fq >> 1) + q;
            const int tck = (rg * 32 + 16 * m) / 8 + (p >> 1);
            const unsigned yb = ldsb + (2 + wc) * KB_STAGE + spare + crow * 128 + 8 * (p & 1);
            TRRD(yv[m][h][0], yb + 16 * (tck ^ (((crow >> 1) & 3) << 1)), 0);
            TRRD(yv[m][h][1], yb + 16 * (tck ^ ((((crow + 4) >> 1) & 3) << 1)), 512);
        }
    asm volatile("s_waitcnt vmcnt(0) lgkmcnt(0)" : "+v"(pin[0][0]), "+v"(pin[0][1]), "+v"(pin[1][0]), "+v"(pin[1][1]), "+v"(yv[0][0][0]), "+v"(yv[0][0][1]), "+v"(yv[0][1][0]), "+v"(yv[0][1][1]),
                 "+v"(yv[1][0][0]), "+v"(yv[1][0][1]), "+v"(yv[1][1][0]), "+v"(yv[1][1][1]) :: "memory");
    const rsrc_t ro = mk_rsrc(P.out, SEQ * DM * 2);
#pragma unroll
    for (int m = 0; m < 2; ++m) {
        const float rs = st[m][1], mrs = -st[m][0] * rs;
#pragma unroll
        for (int h = 0; h < 2; ++h) {
            const u32x4 w = pin[m][h];
            const f32x4 a0 = acc[m][2 * h] + (f32x4){bf_lo(w[0]), bf_hi(w[0]), bf_lo(w[1]), bf_hi(w[1])};
            const f32x4 a1 = acc[m][2 * h + 1] + (f32x4){bf_lo(w[2]), bf_hi(w[2]), bf_lo(w[3]), bf_hi(w[3])};
            float o[8];
#pragma unroll
            for (int j = 0; j < 4; ++j) {
                const auto sw = __builtin_amdgcn_permlane16_swap(__float_as_uint(a0[j]), __float_as_uint(a1[j]), false, false);
                o[j] = __uint_as_float(sw[0]); o[4 + j] = __uint_as_float(sw[1]);
            }
#pragma unroll
            for (int j = 0; j < 8; ++j) {
                const unsigned xw = xv[m][h][j >> 1];
                const float xf = (j & 1) ? bf_hi(xw) : bf_lo(xw);
                const float yf = __uint_as_float((unsigned)(u16)yv[m][h][j >> 2][j & 3] << 16);
                o[j] += xf + (xf * rs + mrs) * yf;
            }
            st16(ro, (unsigned)(((erow + 16 * m) * DM + ecol + 32 * h) * 2), (u32x4){pack_bf16(o[0], o[1]), pack_bf16(o[2], o[3]), pack_bf16(o[4], o[5]), pack_bf16(o[6], o[7])});
        }
    }
    ST();
#undef KE_M
#undef KE_WAITL
#undef KE_STEP
#undef KD_ADDR
#undef KD_NEXT
}

constexpr int PA_K = 0, PA_V = 17408, PA_T = 34816, PA_C = 51200;
__global__ __launch_bounds__(512, 2) void fused_kernel(Params P) {
    extern __shared__ __attribute__((aligned(16))) unsigned char shm[];
    LAS unsigned char* lds = (LAS unsigned char*)shm;
    int _sti = 0;
    ST();
    const int tid = threadIdx.x, wid = __builtin_amdgcn_readfirstlane(tid >> 6), lane = tid & 63;
    const int b = blockIdx.x, xcd = b & 7, bi = b >> 3;
    const u64 tag = __hip_atomic_load((gu64*)(P.sync + SY_EPOCH), __ATOMIC_RELAXED, __HIP_MEMORY_SCOPE_AGENT) + 1ull;
    const bool is_gemm = bi < 28;
    const int pm1 = 4 * (xcd & 1) + (bi & 3), pn1 = 7 * (xcd >> 1) + (bi >> 2), cb = xcd * 4 + (bi - 28);
    const G1Addr ga = gemm1_addr(P, pm1, is_gemm ? pn1 : 0);
    ConvPre cpre;
    {
        const int row = b * 8 + wid, cg = b & 15, rt = b >> 4;
        const u16* xr = P.x + (size_t)row * DM;
        u32x4 a, bq; u32x2 wt;
        asm volatile("global_load_dwordx4 %0, %1, off nt" : "=&v"(a) : "v"(xr + lane * 8) : "memory");
        asm volatile("global_load_dwordx4 %0, %1, off offset:1024 nt" : "=&v"(bq) : "v"(xr + lane * 8) : "memory");
        asm volatile("global_load_dwordx2 %0, %1, off" : "=&v"(wt) : "v"(P.shk + (size_t)(cg * 64 + lane) * 4) : "memory");
#pragma unroll
        for (int i = 0; i < 2; ++i) {
            const int j = wid + 8 * i, kr = 128 * rt - 8 + 8 * j + (lane >> 3);
            __builtin_amdgcn_global_load_lds((const GAS unsigned*)(P.kin + (size_t)(kr < 0 ? 0 : kr) * DM + cg * 64 + (lane & 7) * 8), (LAS unsigned*)(lds + PA_K + j * 1024), 16, 0, 2);
            __builtin_amdgcn_global_load_lds((const GAS unsigned*)(P.vin + (size_t)(128 * rt + 8 * j + (lane >> 3)) * DM + cg * 64 + (lane & 7) * 8), (LAS unsigned*)(lds + PA_V + j * 1024), 16, 0, 2);
        }
        if (wid == 0) __builtin_amdgcn_global_load_lds((const GAS unsigned*)(P.kin + (size_t)(128 * rt + 120 + (lane >> 3)) * DM + cg * 64 + (lane & 7) * 8), (LAS unsigned*)(lds + PA_K + 16 * 1024), 16, 0, 2);
        WAIT_V(4);
        asm volatile("" : "+v"(a), "+v"(bq), "+v"(wt));
        float v[16];
#pragma unroll
        for (int i = 0; i < 4; ++i) { v[2 * i] = bf_lo(a[i]); v[2 * i + 1] = bf_hi(a[i]); v[8 + 2 * i] = bf_lo(bq[i]); v[9 + 2 * i] = bf_hi(bq[i]); }
        float s = 0.f;
#pragma unroll
        for (int i = 0; i < 16; ++i) s += v[i];
        const float mean = wave_sum(s) * (1.0f / DM);
        float q = 0.f;
#pragma unroll
        for (int i = 0; i < 16; ++i) { const float d = v[i] - mean; q += d * d; }
        const float mb = bf_round(mean), vb = bf_round(wave_sum(q) * (1.0f / DM));
        const float sb = bf_round(sqrtf(bf_round(vb + 1e-5f)));
        const float rs = 1.0f / sb;
        u32x4 oa, ob;
#pragma unroll
        for (int i = 0; i < 4; ++i) {
            oa[i] = pack_bf16(bf_round(v[2 * i] - mb) * rs, bf_round(v[2 * i + 1] - mb) * rs);
            ob[i] = pack_bf16(bf_round(v[8 + 2 * i] - mb) * rs, bf_round(v[9 + 2 * i] - mb) * rs);
        }
        const float w0 = bf_lo(wt[0]), w1 = bf_hi(wt[0]), w2 = bf_lo(wt[1]), w3 = bf_hi(wt[1]);
        ST();
        const rsrc_t rx = mk_rsrc(P.xn, SEQ * DM * 2);
        st16(rx, (unsigned)(row * DM * 2 + lane * 16), oa); st16(rx, (unsigned)(row * DM * 2 + 1024 + lane * 16), ob);
        st8(mk_rsrc(P.stats, SEQ * 8), lane == 0 ? (unsigned)(row * 8) : 0xfffffff0u, (u32x2){__float_as_uint(mb), __float_as_uint(rs)});
        if (tid == 0) { *(LAS unsigned*)(lds + PA_C) = 0u; *(LAS unsigned*)(lds + PA_C + 4) = 0u; }
        if (is_gemm) { WAIT_V(3); } else { conv_prefetch(P, cb, cpre); WAIT_V(7); }
        WAIT_L(0); BAR();
        if (is_gemm) { STAGE_B(SB(0, 0), ga.cG, 0); STAGE_B(SB(0, 1), ga.cU, 0); STAGE_B(SB(1, 0), ga.cG, 1); STAGE_B(SB(1, 1), ga.cU, 1); }
        ST();
        float kf[19], kv[16];
#pragma unroll
        for (int i = 0; i < 19; ++i) kf[i] = bf_lo((unsigned)*(const LAS u16*)(lds + PA_K + (16 * wid + 5 + i) * 128 + lane * 2));
        if (rt == 0 && wid == 0) { kf[0] = 0.f; kf[1] = 0.f; kf[2] = 0.f; }
#pragma unroll
        for (int i = 0; i < 16; ++i) {
            const float vf = bf_lo((unsigned)*(const LAS u16*)(lds + PA_V + (16 * wid + i) * 128 + lane * 2));
            kv[i] = (kf[i] * w3 + kf[i + 1] * w2 + kf[i + 2] * w1 + kf[i + 3] * w0) * vf;
        }
#pragma unroll
        for (int j = 0; j < 2; ++j) {
            u32x4 o;
#pragma unroll
            for (int i = 0; i < 4; ++i) o[i] = pack_bf16(kv[8 * j + 7 - 2 * i], kv[8 * j + 6 - 2 * i]);
            *(LAS u32x4*)(lds + PA_T + lane * 256 + 16 * ((2 * wid + j) ^ (lane & 15))) = o;
        }
        ST();
        WAIT_L(0); BAR();
        const rsrc_t rk = mk_rsrc(P.kvT, SEQ * DM * 2);
#pragma unroll
        for (int i = 0; i < 2; ++i) {
            const int ch = 8 * wid + 4 * i + (lane >> 4), ck = lane & 15;
            st16(rk, (unsigned)(((cg * 64 + ch) * SEQ + 128 * rt + 8 * ck) * 2), *(const LAS u32x4*)(lds + PA_T + ch * 256 + 16 * (ck ^ (ch & 15))));
        }
        if (is_gemm) { WAIT_V(10); } else { WAIT_V(6); }
        if (last_wave((LAS unsigned*)(lds + PA_C), 8) && lane == 0) signal(P.sync + SY_X + b, tag);
        WAIT_V(0);
        if (last_wave((LAS unsigned*)(lds + PA_C + 4), 8) && lane == 0) signal(P.sync + SY_K + (b & 15) * 16 + (b >> 4), tag);
        ST();
    }
    ST();
    const int ks = xcd & 1, pm2 = 8 * ((xcd >> 1) & 1) + (bi & 7), pn2 = 4 * (xcd >> 2) + (bi >> 3);
    const G2Load g2 = gemm2_addr(P, ks, pm2, pn2, wid & 3, lane);
    if (is_gemm) {
        if (wid == 0) poll_words(P.sync + SY_X + pm1 * 32 + (lane & 31), true, tag);
        BAR();
        STAGE_A(SA(0, 0), 0, 0); STAGE_A(SA(0, 1), 1, 0); STAGE_A(SA(1, 0), 0, 1);
        ST();
        gemm1_block(P, lds, ga, g2, ks, pm1, pn1, _sti);
    } else {
        if (wid == 0) poll_words(P.sync + SY_K + (cb >> 1) * 16 + (lane & 15), true, tag);
        BAR();
        ST();
        ST();
        asm volatile("" : "+v"(cpre.nh[0]), "+v"(cpre.nh[1]), "+v"(cpre.nh[2]), "+v"(cpre.nh[3]));
        conv_block(P, lds, cb, cpre, _sti);
        WAIT_L(0); BAR();
        if (tid == 0) *(LAS unsigned*)(lds + SYNC_CNT) = 0u;
        WAIT_L(0); BAR();
        if (wid >= 4) { const int lw = wid - 4; KD_ISSUE1B(0, 0); KD_ISSUE1B(1, 1); KD_ISSUE1B(2, 2); KD_ISSUE1B(3, 3); }
        ST();
    }
    if (wid >= 4 && !is_gemm) { WAIT_V(16); } else { WAIT_V(0); }
    if (last_wave((LAS unsigned*)(lds + SYNC_CNT), 8) && lane == 0) signal(is_gemm ? P.sync + SY_H + pm1 * 32 + pn1 : P.sync + SY_Y + cb, tag);
    if (wid == 4) poll_words(lane < 28 ? P.sync + SY_H + (pm2 >> 1) * 32 + lane : P.sync + SY_Y + 4 * pn2 + (lane & 3), lane < 32, tag);
    WAIT_L(0); BAR();
    ST();
    if (wid >= 4) {
        gemm2_loader(P, lds, g2, ks, pm2, pn2, wid - 4, lane);
        if (b == 0 && wid == 4) {
#pragma unroll
            for (int i = 0; i < 4; ++i) poll_words(P.sync + SY_X + i * 64 + lane, true, tag);
            if (lane == 0) signal(P.sync + SY_EPOCH, tag);
        }
    } else {
        gemm2_mult(P, lds, ks, pm2, pn2, wid, lane, tag, _sti);
    }
}

extern "C" void kernel_launch(void* const* d_in, const int* in_sizes, int n_in, void* d_out, int out_size, void* d_ws, size_t ws_size, hipStream_t stream) {
    unsigned char* ws = (unsigned char*)d_ws;
    const size_t MB = 1u << 20;
    Params P;
    P.x = (const u16*)d_in[0]; P.kin = (const u16*)d_in[1]; P.vin = (const u16*)d_in[2]; P.shk = (const u16*)d_in[3]; P.ssmk = (const u16*)d_in[4]; P.ssmD = (const u16*)d_in[5];
    P.wg = (const u16*)d_in[7]; P.wu = (const u16*)d_in[8]; P.wd = (const u16*)d_in[9];
    P.out = (u16*)d_out;
    P.xn = (u16*)(ws + 0 * MB); P.kvT = (u16*)(ws + 4 * MB); P.y2T = (u16*)(ws + 8 * MB); P.hbuf = (u16*)(ws + 16 * MB);
    P.stats = (f32x2*)(ws + 12 * MB); P.part = (float*)(ws + 32 * MB); P.sync = (u64*)(ws + 13 * MB); P.stamps = (u64*)(ws + 64 * MB);
    (void)hipFuncSetAttribute((const void*)fused_kernel, hipFuncAttributeMaxDynamicSharedMemorySize, LDS_BYTES);
    fused_kernel<<<256, 512, LDS_BYTES, stream>>>(P);
}
```

```cpp
#include <hip/hip_runtime.h>
#include <hip/hip_bf16.h>
#include <stdint.h>

#define LAS __attribute__((address_space(3)))
#define GAS __attribute__((address_space(1)))
typedef unsigned short u16;
typedef short bf16x8 __attribute__((ext_vector_type(8)));
typedef short bf16x4 __attribute__((ext_vector_type(4)));
typedef float f32x4 __attribute__((ext_vector_type(4)));
typedef float f32x2 __attribute__((ext_vector_type(2)));
typedef unsigned u32x4 __attribute__((ext_vector_type(4)));
typedef unsigned u32x2 __attribute__((ext_vector_type(2)));
typedef __bf16 bf16x2_t __attribute__((ext_vector_type(2)));
typedef unsigned long long u64;
typedef GAS u64 gu64;

constexpr int SEQ = 2048, DM = 1024, FF = 3584;

__device__ __forceinline__ unsigned pack_bf16(float lo, float hi) {
    f32x2 v = {lo, hi};
    return __builtin_bit_cast(unsigned, __builtin_convertvector(v, bf16x2_t));
}
__device__ __forceinline__ float bf_lo(unsigned u) { return __uint_as_float(u << 16); }
__device__ __forceinline__ float bf_hi(unsigned u) { return __uint_as_float(u & 0xffff0000u); }
__device__ __forceinline__ float bf_round(float f) { return bf_lo(pack_bf16(f, f)); }

__device__ __forceinline__ float wave_sum(float v) {
    v += __builtin_bit_cast(float, __builtin_amdgcn_update_dpp(0, __builtin_bit_cast(int, v), 0x111, 0xf, 0xf, true));
    v += __builtin_bit_cast(float, __builtin_amdgcn_update_dpp(0, __builtin_bit_cast(int, v), 0x112, 0xf, 0xf, true));
    v += __builtin_bit_cast(float, __builtin_amdgcn_update_dpp(0, __builtin_bit_cast(int, v), 0x114, 0xf, 0xf, true));
    v += __builtin_bit_cast(float, __builtin_amdgcn_update_dpp(0, __builtin_bit_cast(int, v), 0x118, 0xf, 0xf, true));
    v += __builtin_bit_cast(float, __builtin_amdgcn_update_dpp(0, __builtin_bit_cast(int, v), 0x142, 0xa, 0xf, true));
    v += __builtin_bit_cast(float, __builtin_amdgcn_update_dpp(0, __builtin_bit_cast(int, v), 0x143, 0xc, 0xf, true));
    return __builtin_bit_cast(float, __builtin_amdgcn_readlane(__builtin_bit_cast(int, v), 63));
}
typedef __amdgpu_buffer_rsrc_t rsrc_t;
__device__ __forceinline__ rsrc_t mk_rsrc(const void* base, unsigned bytes) { return __builtin_amdgcn_make_buffer_rsrc((void*)base, 0, bytes, 0x00020000); }
__device__ __forceinline__ void st16(rsrc_t r, unsigned off, u32x4 v) { __builtin_amdgcn_raw_buffer_store_b128(v, r, off, 0, 16); }
__device__ __forceinline__ void st8(rsrc_t r, unsigned off, u32x2 v) { __builtin_amdgcn_raw_buffer_store_b64(v, r, off, 0, 16); }
__device__ __forceinline__ u32x4 ld16_stream(const void* p) { return __builtin_nontemporal_load((const u32x4*)p); }

#define WAIT_V(n) asm volatile("s_waitcnt vmcnt(" #n ")" ::: "memory")
#define WAIT_L(n) asm volatile("s_waitcnt lgkmcnt(" #n ")" ::: "memory")
#define BAR() __builtin_amdgcn_s_barrier()
#define SCHED() __builtin_amdgcn_sched_barrier(0)
#define GLDS(gptr, ldsptr) __builtin_amdgcn_global_load_lds((const GAS unsigned*)(gptr), (LAS unsigned*)(ldsptr), 16, 0, 0)
#define DEV_AUX 16
#define GLDS_DEV(gptr, ldsptr) __builtin_amdgcn_global_load_lds((const GAS unsigned*)(gptr), (LAS unsigned*)(ldsptr), 16, 0, DEV_AUX)
#define TRRD(dst, addr, off) asm volatile("ds_read_b64_tr_b16 %0, %1 offset:%c2" : "=&v"(dst) : "v"(addr), "i"(off) : "memory")
#define RD128(dst, addr, off) asm volatile("ds_read_b128 %0, %1 offset:%c2" : "=&v"(dst) : "v"(addr), "i"(off) : "memory")
#define CAT(lo, hi) __builtin_shufflevector(lo, hi, 0, 1, 2, 3, 4, 5, 6, 7)

#define X_POL ""
#define ST()

struct Params {
    const u16 *x, *kin, *vin, *shk, *ssmk, *ssmD, *wg, *wu, *wd;
    u16 *out, *xn, *kvT, *y2T, *hbuf;
    f32x2* stats;
    float* part;
    u64* sync;
    u64* stamps;
};
constexpr int SY_EPOCH = 0, SY_X = 128, SY_K = 512, SY_H = 1024, SY_Y = 1536, SY_P = 2048;

__device__ __forceinline__ void signal(u64* word, u64 tag) { __hip_atomic_store((gu64*)word, tag, __ATOMIC_RELAXED, __HIP_MEMORY_SCOPE_AGENT); }
__device__ __forceinline__ void poll_words(const u64* word, bool want, u64 tag) {
    const u64 t0 = __builtin_amdgcn_s_memrealtime();
    for (;;) {
        const u64 v = want ? __hip_atomic_load((gu64*)word, __ATOMIC_RELAXED, __HIP_MEMORY_SCOPE_AGENT) : tag;
        if (__all(v == tag)) break;
        if (__builtin_amdgcn_s_memrealtime() - t0 > 5000000ull) break;
        __builtin_amdgcn_s_sleep(2);
    }
}
__device__ __forceinline__ bool last_wave(LAS unsigned* cnt, unsigned n) {
    unsigned old = 0; unsigned long long ex; const unsigned addr = (unsigned)(uintptr_t)cnt, one = 1u;
    asm volatile("s_mov_b64 %1, exec\n\ts_mov_b64 exec, 1\n\tds_add_rtn_u32 %0, %2, %3\n\ts_waitcnt lgkmcnt(0)\n\ts_mov_b64 exec, %1" : "+v"(old), "=&s"(ex) : "v"(addr), "v"(one) : "memory");
    return (unsigned)__builtin_amdgcn_readfirstlane(old) == n - 1;
}

constexpr int CV_XB0 = 512, CV_XB1 = 4608 + 512, CV_HE = 9216, CV_HO = 9216 + 4160, CV_WAVE = 20480;
struct ConvPre { u32x4 nh[4]; };
__device__ __forceinline__ void conv_prefetch(const Params& P, int cb, ConvPre& pre) {
    const int wid = __builtin_amdgcn_readfirstlane(threadIdx.x >> 6), lane = threadIdx.x & 63;
#pragma unroll
    for (int i = 0; i < 4; ++i) { const u16* q = P.ssmk + (size_t)(cb * 32 + wid * 4) * SEQ + (i * 64 + lane) * 8; asm volatile("global_load_dwordx4 %0, %1, off nt" : "=&v"(pre.nh[i]) : "v"(q) : "memory"); }
}
__device__ __forceinline__ void conv_block(const Params& P, LAS unsigned char* lds, int cb, ConvPre& pre, int& _sti) {
    const int tid = threadIdx.x, c0 = cb * 32;
    const int wid = __builtin_amdgcn_readfirstlane(tid >> 6), lane = tid & 63;
    LAS unsigned char* wl = lds + wid * CV_WAVE;
    const int chw = c0 + wid * 4;
    const rsrc_t rkv = mk_rsrc(P.kvT, SEQ * DM * 2); const u16* ssmk = P.ssmk;
    u32x4 nx[4], nh[4], no[4];
#define CV_GX(ch) do { _Pragma("unroll") for (int i = 0; i < 4; ++i) nx[i] = __builtin_amdgcn_raw_buffer_load_b128(rkv, (unsigned)(((ch) * SEQ + (i * 64 + lane) * 8) * 2), 0, 16); } while (0)
#define CV_GH(ch) do { _Pragma("unroll") for (int i = 0; i < 4; ++i) nh[i] = ld16_stream(ssmk + (size_t)(ch) * SEQ + (i * 64 + lane) * 8); } while (0)
    CV_GX(chw);
    if (lane < 32) { *(LAS u32x4*)(wl + lane * 16) = (u32x4){0u, 0u, 0u, 0u}; *(LAS u32x4*)(wl + 4608 + lane * 16) = (u32x4){0u, 0u, 0u, 0u}; }
    if (lane < 2) { *(LAS u32x4*)(wl + CV_HE + lane * 16) = (u32x4){0u, 0u, 0u, 0u}; *(LAS u32x4*)(wl + CV_HO + lane * 16) = (u32x4){0u, 0u, 0u, 0u}; }
    const int r = lane & 15, g = lane >> 4, g0 = g & 1, gh = g >> 1;
    const int s0 = r - 8 * g0 + 9 + 16 * gh;
    const int aoff = (r & 1) ? (CV_HE + 2 * s0) : (CV_HO + 2 * (s0 + 1));
    const int boff = 32 * (r - gh) + 16 * g0 - 448;
    const rsrc_t ry = mk_rsrc(P.y2T, SEQ * DM * 2);
    LAS unsigned char* abase[4];
#pragma unroll
    for (int i = 0; i < 4; ++i) { unsigned a = (unsigned)(uintptr_t)(wl + aoff + 1024 * i); asm volatile("" : "+v"(a)); abase[i] = (LAS unsigned char*)(uintptr_t)a; }
    unsigned dmask[4];
#pragma unroll
    for (int i = 0; i < 4; ++i) dmask[i] = (gh == 0 && 8 * g0 + 7 - 2 * i == r) ? 0x0000ffffu : (gh == 0 && 8 * g0 + 6 - 2 * i == r) ? 0xffff0000u : 0u;
    const u32x2 dpair = *(const u32x2*)(P.ssmD + chw);
#define CV_PUTX(par) do { _Pragma("unroll") for (int i = 0; i < 4; ++i) *(LAS u32x4*)(wl + ((par) ? CV_XB1 : CV_XB0) + (i * 64 + lane) * 16) = nx[i]; } while (0)
#define CV_SHIFT() do { unsigned rot[4]; \
        _Pragma("unroll") for (int i = 0; i < 4; ++i) rot[i] = (unsigned)__builtin_amdgcn_update_dpp(0, (int)nh[i][3], 0x13C  , 0xf, 0xf, false); \
        _Pragma("unroll") for (int i = 0; i < 4; ++i) { \
            const unsigned prev = (lane == 0) ? (i == 0 ? 0u : rot[i > 0 ? i - 1 : 0]) : rot[i]; \
            no[i][0] = __builtin_amdgcn_alignbit(nh[i][0], prev, 16); no[i][1] = __builtin_amdgcn_alignbit(nh[i][1], nh[i][0], 16); \
            no[i][2] = __builtin_amdgcn_alignbit(nh[i][2], nh[i][1], 16); no[i][3] = __builtin_amdgcn_alignbit(nh[i][3], nh[i][2], 16); } } while (0)
#define CV_PUTH() do { _Pragma("unroll") for (int i = 0; i < 4; ++i) { \
            *(LAS u32x4*)(wl + CV_HE + 32 + (i * 64 + lane) * 16) = nh[i]; *(LAS u32x4*)(wl + CV_HO + 32 + (i * 64 + lane) * 16) = no[i]; } } while (0)
#define CV_LOAD(buf, par, v) do { _Pragma("unroll") for (int u = 0; u < 8; ++u) { \
            bf[buf][u] = *(const LAS bf16x8*)(wl + ((par) ? CV_XB1 : CV_XB0) + boff + 512 * u + 64 * (7 - (v))); \
            const LAS unsigned* pa = (const LAS unsigned*)(abase[u >> 1] + 64 * (8 * (u & 1) + (v))); \
            u32x4 t; t[0] = pa[0]; t[1] = pa[1]; t[2] = pa[2]; t[3] = pa[3]; \
            af[buf][u] = __builtin_bit_cast(bf16x8, t); } } while (0)
#define CV_EPI(a, ch, c) do { u32x2 o; o[0] = pack_bf16(acc[a][c][0], acc[a][c][1]); o[1] = pack_bf16(acc[a][c][2], acc[a][c][3]); \
            st8(ry, (unsigned)(((ch) * SEQ + 256 * (c) + 16 * r + 4 * g) * 2), o); } while (0)
    bf16x8 af[2][8], bf[2][8];
    f32x4 acc[2][8];
    unsigned touch = 0;
#pragma unroll
    for (int i = 0; i < 4; ++i) nh[i] = pre.nh[i];
    CV_SHIFT(); CV_PUTH();
    CV_PUTX(0);
    CV_LOAD(0, 0, 0);
    CV_GX(chw + 1); CV_GH(chw + 1);
    SCHED();
#pragma unroll
    for (int cc = 0; cc < 4; ++cc) {
        const int a = cc & 1;
        if (cc == 0) __builtin_amdgcn_s_setprio(3); else if (cc == 1) __builtin_amdgcn_s_setprio(2); else if (cc == 2) __builtin_amdgcn_s_setprio(1); else __builtin_amdgcn_s_setprio(0);
        const unsigned dw = dpair[cc >> 1], dd = (cc & 1) ? ((dw & 0xffff0000u) | (dw >> 16)) : ((dw << 16) | (dw & 0xffffu));
        const u32x4 adw = {dd & dmask[0], dd & dmask[1], dd & dmask[2], dd & dmask[3]};
        const bf16x8 ad = __builtin_bit_cast(bf16x8, adw);
#pragma unroll
        for (int v = 0; v < 8; ++v) {
            const int cur = v & 1, nxt = cur ^ 1;
            if (v < 7) CV_LOAD(nxt, a, v + 1);
            else if (cc < 3) { CV_PUTH(); CV_LOAD(nxt, a ^ 1, 0); }
            if (v == 4 && cc < 3) CV_PUTX(a ^ 1);
            if (v == 5 && cc < 3) { CV_SHIFT(); if (cc < 2) CV_GX(chw + cc + 2); }
            if (v == 0 && cc > 0 && cc < 3) CV_GH(chw + cc + 1);
            if (v == 3) {
                const int ln = ((cb * 8 + wid) * 4 + cc) * 64 + lane;
                const char* q = (const char*)P.wd + (size_t)(ln < FF * 16 ? ln : 0) * 128;
                asm volatile("global_load_dword %0, %1, off" : "+v"(touch) : "v"(q) : "memory");
            }
            if (v == 0) {
#pragma unroll
                for (int c = 0; c < 8; ++c) acc[a][c] = __builtin_amdgcn_mfma_f32_16x16x32_bf16(ad, bf[cur][c], (f32x4){0.f, 0.f, 0.f, 0.f}, 0, 0, 0);
            }
#pragma unroll
            for (int u = 0; u < 8; ++u)
#pragma unroll
                for (int c = u; c < 8; ++c) acc[a][c] = __builtin_amdgcn_mfma_f32_16x16x32_bf16(af[cur][u], bf[cur][c - u], acc[a][c], 0, 0, 0);
            if (cc > 0 && v >= 1 && v < 3) {
#pragma unroll
                for (int c = 0; c < 4; ++c) CV_EPI(a ^ 1, chw + cc - 1, 4 * (v - 1) + c);
            }
            if (v == 7) {
                if (cc < 3) {
#pragma unroll
                    for (int i = 0; i < 8; ++i) { __builtin_amdgcn_sched_group_barrier(0x008, 1, 0); __builtin_amdgcn_sched_group_barrier(0x200, 1, 0); }
#pragma unroll
                    for (int i = 0; i < 24; ++i) { __builtin_amdgcn_sched_group_barrier(0x008, 1, 0); __builtin_amdgcn_sched_group_barrier(0x100, 1, 0); }
                }
            } else {
#pragma unroll
                for (int i = 0; i < 24; ++i) { __builtin_amdgcn_sched_group_barrier(0x008, 1, 0); __builtin_amdgcn_sched_group_barrier(0x100, 1, 0); }
                if (v == 0 && cc > 0 && cc < 3) {
#pragma unroll
                    for (int i = 0; i < 4; ++i) { __builtin_amdgcn_sched_group_barrier(0x008, 1, 0); __builtin_amdgcn_sched_group_barrier(0x020, 1, 0); }
                }
                if (cc > 0 && v >= 1 && v < 3) {
#pragma unroll
                    for (int i = 0; i < 4; ++i) { __builtin_amdgcn_sched_group_barrier(0x008, 1, 0); __builtin_amdgcn_sched_group_barrier(0x002, 2, 0); __builtin_amdgcn_sched_group_barrier(0x008, 1, 0); __builtin_amdgcn_sched_group_barrier(0x040, 1, 0); }
                }
                if (v == 4 && cc < 3) {
#pragma unroll
                    for (int i = 0; i < 4; ++i) { __builtin_amdgcn_sched_group_barrier(0x008, 1, 0); __builtin_amdgcn_sched_group_barrier(0x200, 1, 0); }
                }
                if (v == 5 && cc < 3) {
#pragma unroll
                    for (int i = 0; i < 12; ++i) { __builtin_amdgcn_sched_group_barrier(0x008, 1, 0); __builtin_amdgcn_sched_group_barrier(0x002, 2, 0); }
                }
            }
            SCHED();
        }
    }
#pragma unroll
    for (int c = 0; c < 8; ++c) CV_EPI(1, chw + 3, c);
    __builtin_amdgcn_s_setprio(0);
    WAIT_V(0); asm volatile("" :: "v"(touch));
    ST();
#undef CV_GX
#undef CV_GH
#undef CV_PUTX
#undef CV_SHIFT
#undef CV_PUTH
#undef CV_LOAD
#undef CV_EPI
}

constexpr int KB_STAGE = 32768, KC_N1 = 23, LDS_BYTES = 5 * KB_STAGE;
constexpr int SYNC_CNT = 5 * 32768 - 16;
struct G2Load { unsigned vA1, vA2, vB0, vB1; const char *cA, *cB; };
#define KD_B4(pb, pl) do { GLDS((pb) + g2.vB0, (pl) + 16384 + lw * 4096); GLDS((pb) + g2.vB0 + (size_t)4 * DM * 2, (pl) + 16384 + lw * 4096 + 1024); \
        GLDS((pb) + g2.vB1, (pl) + 16384 + lw * 4096 + 2048); GLDS((pb) + g2.vB1 + (size_t)4 * DM * 2, (pl) + 16384 + lw * 4096 + 3072); } while (0)
#define KD_ISSUE1A(s_, slot) do { const char* _a = g2.cA + (size_t)(KC_N1 * ks + (s_)) * 128 + g2.vA1; LAS unsigned char* _l = lds + (slot) * KB_STAGE; \
        GLDS_DEV(_a, _l + lw * 4096); GLDS_DEV(_a + (size_t)8 * FF * 2, _l + lw * 4096 + 1024); GLDS_DEV(_a + (size_t)16 * FF * 2, _l + lw * 4096 + 2048); GLDS_DEV(_a + (size_t)24 * FF * 2, _l + lw * 4096 + 3072); } while (0)
#define KD_ISSUE1B(s_, slot) do { const char* _b = g2.cB + (size_t)(KC_N1 * ks + (s_)) * 64 * DM * 2; KD_B4(_b, lds + (slot) * KB_STAGE); } while (0)
#define KD_ISSUE1(s_, slot) do { KD_ISSUE1A(s_, slot); KD_ISSUE1B(s_, slot); } while (0)
#define KD_ISSUE2(s_, slot) do { const int _kt = KC_N1 + (s_); const char* _a = g2.cA + (size_t)_kt * 128 + g2.vA2; const char* _b = g2.cB + (size_t)_kt * 64 * DM * 2; LAS unsigned char* _l = lds + (slot) * KB_STAGE; \
        GLDS_DEV(_a, _l + ks * 8192 + lw * 2048); GLDS_DEV(_a + (size_t)8 * FF * 2, _l + ks * 8192 + lw * 2048 + 1024); KD_B4(_b, _l); } while (0)
__device__ __forceinline__ G2Load gemm2_addr(const Params& P, int ks, int pm, int pn, int lw, int lane) {
    G2Load g2;
    const unsigned swA = (unsigned)(8 * ((lane & 7) ^ ((lane >> 3) & 7)) * 2);
    g2.vA1 = (unsigned)((lw * 32 + (lane >> 3)) * FF * 2) + swA;
    g2.vA2 = (unsigned)((ks * 64 + lw * 16 + (lane >> 3)) * FF * 2) + swA;
    g2.vB0 = (unsigned)(((lw * 16 + (lane >> 4)) * DM + 8 * ((lane & 15) ^ (((lane >> 4) & 3) << 2))) * 2);
    g2.vB1 = (unsigned)(((lw * 16 + 8 + (lane >> 4)) * DM + 8 * ((lane & 15) ^ ((((lane >> 4) & 3) << 2) | 2))) * 2);
    g2.cA = (const char*)P.hbuf + (size_t)pm * 128 * FF * 2;
    g2.cB = (const char*)P.wd + (size_t)pn * 128 * 2;
    return g2;
}

constexpr int HTB = 16384;
#define SA(b, h) (((b) * 2 + (h)) * HTB)
#define SB(b, h) ((4 + (b) * 2 + (h)) * HTB)
struct G1Addr { unsigned voffA, voffB; const char *cA, *cG, *cU; unsigned ldsw; };
#define STAGE_A(bufoff, h, kt) do { const char* _g = ga.cA + (h) * ((size_t)128 * DM * 2) + (size_t)(kt) * 128; \
        GLDS_DEV(_g + ga.voffA, lds + (bufoff) + ga.ldsw); GLDS_DEV(_g + (size_t)64 * DM * 2 + ga.voffA, lds + (bufoff) + ga.ldsw + 8192); } while (0)
#define STAGE_B(bufoff, base, kt) do { const char* _g = (base) + (size_t)(kt) * ((size_t)64 * FF * 2); \
        GLDS(_g + ga.voffB, lds + (bufoff) + ga.ldsw); GLDS(_g + (size_t)32 * FF * 2 + ga.voffB, lds + (bufoff) + ga.ldsw + 8192); } while (0)
__device__ __forceinline__ G1Addr gemm1_addr(const Params& P, int pm, int pn) {
    const int tid = threadIdx.x, wid = __builtin_amdgcn_readfirstlane(tid >> 6);
    G1Addr ga;
    ga.voffA = (unsigned)(((tid >> 3) * DM + 8 * ((tid & 7) ^ ((tid >> 3) & 7))) * 2);
    const int kb = tid >> 4;
    ga.voffB = (unsigned)((kb * FF + 8 * ((tid & 15) ^ (((kb & 3) << 2) | (((kb >> 3) & 1) << 1)))) * 2);
    ga.cA = (const char*)P.xn + (size_t)pm * 256 * DM * 2;
    ga.cG = (const char*)P.wg + (size_t)pn * 128 * 2;
    ga.cU = (const char*)P.wu + (size_t)pn * 128 * 2;
    ga.ldsw = (unsigned)wid * 1024u;
    return ga;
}
__device__ __forceinline__ void gemm1_block(const Params& P, LAS unsigned char* lds, const G1Addr& ga, const G2Load& g2, int ks, int pm, int pn, int& _sti) {
    const int tid = threadIdx.x, wid = __builtin_amdgcn_readfirstlane(tid >> 6), lane = tid & 63, wr = wid >> 2, wc = wid & 3, fr = lane & 15, fq = lane >> 4;
    constexpr int nt = DM / 64;
    const int q = (lane & 15) >> 2, p = lane & 3;
    const int aoff0 = (wr * 64 + fr) * 128 + 16 * ((fq) ^ (fr & 7)), aoff1 = (wr * 64 + fr) * 128 + 16 * ((4 + fq) ^ (fr & 7));
    const int boff0 = 256 * (8 * fq + q) + 16 * (4 * (wc ^ q) + 2 * (0 ^ (fq & 1)) + (p >> 1)) + 8 * (p & 1);
    const int boff1 = 256 * (8 * fq + q) + 16 * (4 * (wc ^ q) + 2 * (1 ^ (fq & 1)) + (p >> 1)) + 8 * (p & 1);
#define LDA(dst, b, h) do { _Pragma("unroll") for (int m = 0; m < 4; ++m) { \
        dst[m][0] = *(const LAS bf16x8*)(lds + SA(b, h) + aoff0 + m * 2048); dst[m][1] = *(const LAS bf16x8*)(lds + SA(b, h) + aoff1 + m * 2048); } } while (0)
#define LDB(dst, b, h) do { \
        TRRD(dst[0][0][0], bb0, SB(b, h) - SB(0, 0)); TRRD(dst[0][0][1], bb0, SB(b, h) - SB(0, 0) + 1024); TRRD(dst[1][0][0], bb1, SB(b, h) - SB(0, 0)); TRRD(dst[1][0][1], bb1, SB(b, h) - SB(0, 0) + 1024); \
        TRRD(dst[0][1][0], bb0, SB(b, h) - SB(0, 0) + 8192); TRRD(dst[0][1][1], bb0, SB(b, h) - SB(0, 0) + 9216); TRRD(dst[1][1][0], bb1, SB(b, h) - SB(0, 0) + 8192); TRRD(dst[1][1][1], bb1, SB(b, h) - SB(0, 0) + 9216); } while (0)
#define WAIT_LB(dst) asm volatile("s_waitcnt lgkmcnt(0)" : "+v"(dst[0][0][0]), "+v"(dst[0][0][1]), "+v"(dst[0][1][0]), "+v"(dst[0][1][1]), "+v"(dst[1][0][0]), "+v"(dst[1][0][1]), "+v"(dst[1][1][0]), "+v"(dst[1][1][1]) :: "memory")
#define MMA(ai, bj, At, Bt) do { __builtin_amdgcn_s_setprio(1); _Pragma("unroll") for (int m = 0; m < 4; ++m) _Pragma("unroll") for (int n = 0; n < 2; ++n) _Pragma("unroll") for (int k = 0; k < 2; ++k) \
        acc[ai][bj][m][n] = __builtin_amdgcn_mfma_f32_16x16x32_bf16(CAT(Bt[n][k][0], Bt[n][k][1]), At[m][k], acc[ai][bj][m][n], 0, 0, 0); __builtin_amdgcn_s_setprio(0); } while (0)
    f32x4 acc[2][2][4][2];
#pragma unroll
    for (int a = 0; a < 2; ++a)
#pragma unroll
        for (int b = 0; b < 2; ++b)
#pragma unroll
            for (int m = 0; m < 4; ++m)
#pragma unroll
                for (int n = 0; n < 2; ++n) acc[a][b][m][n] = (f32x4){0.f, 0.f, 0.f, 0.f};
    bf16x8 At[4][2]; bf16x4 B0[2][2][2], B1[2][2][2];
    const unsigned bb0 = (unsigned)(uintptr_t)(lds + SB(0, 0) + boff0), bb1 = (unsigned)(uintptr_t)(lds + SB(0, 0) + boff1);
    if (wr == 1) BAR();
    WAIT_V(2); WAIT_L(0); BAR(); BAR();
    ST();
    for (int t = 0; t < nt - 2; t += 2) {
        LDB(B0, 0, 0); SCHED(); LDA(At, 0, 0); STAGE_A(SA(1, 1), 1, t + 1);
        WAIT_L(8); BAR(); WAIT_LB(B0); SCHED(); MMA(0, 0, At, B0); BAR(); SCHED();
        LDB(B1, 0, 1); STAGE_B(SB(0, 0), ga.cG, t + 2);
        BAR(); WAIT_LB(B1); SCHED(); MMA(0, 1, At, B1); BAR();
        LDA(At, 0, 1); STAGE_A(SA(0, 0), 0, t + 2);
        BAR(); WAIT_L(0); MMA(1, 0, At, B0); BAR(); SCHED();
        STAGE_B(SB(0, 1), ga.cU, t + 2);
        WAIT_V(6); BAR(); MMA(1, 1, At, B1); BAR();
        LDB(B0, 1, 0); SCHED(); LDA(At, 1, 0); STAGE_A(SA(0, 1), 1, t + 2);
        WAIT_L(8); BAR(); WAIT_LB(B0); SCHED(); MMA(0, 0, At, B0); BAR(); SCHED();
        LDB(B1, 1, 1); STAGE_B(SB(1, 0), ga.cG, t + 3);
        BAR(); WAIT_LB(B1); SCHED(); MMA(0, 1, At, B1); BAR();
        LDA(At, 1, 1); STAGE_A(SA(1, 0), 0, t + 3);
        BAR(); WAIT_L(0); MMA(1, 0, At, B0); BAR(); SCHED();
        STAGE_B(SB(1, 1), ga.cU, t + 3);
        WAIT_V(6); BAR(); MMA(1, 1, At, B1); BAR();
    }
    {
        LDB(B0, 0, 0); SCHED(); LDA(At, 0, 0); STAGE_A(SA(1, 1), 1, nt - 1);
        WAIT_L(8); BAR(); WAIT_LB(B0); SCHED(); MMA(0, 0, At, B0); BAR(); SCHED();
        LDB(B1, 0, 1); BAR(); WAIT_LB(B1); SCHED(); MMA(0, 1, At, B1); BAR();
        LDA(At, 0, 1); BAR(); WAIT_L(0); MMA(1, 0, At, B0); BAR(); SCHED();
        WAIT_V(0); BAR(); MMA(1, 1, At, B1); BAR();
    }
    {
        LDB(B0, 1, 0); SCHED(); LDA(At, 1, 0); BAR(); WAIT_LB(B0); SCHED(); MMA(0, 0, At, B0); BAR(); SCHED();
        LDB(B1, 1, 1); BAR(); WAIT_LB(B1); SCHED(); MMA(0, 1, At, B1); BAR();
        LDA(At, 1, 1); BAR(); WAIT_L(0); MMA(1, 0, At, B0); MMA(1, 1, At, B1);
    }
    if (tid == 0) *(LAS unsigned*)(lds + SYNC_CNT) = 0u;
    if (wr == 0) BAR();
    WAIT_L(0); BAR();
    ST();
    if (wid >= 4) { const int lw = wid - 4; KD_ISSUE1B(0, 0); KD_ISSUE1B(1, 1); KD_ISSUE1B(2, 2); KD_ISSUE1B(3, 3); }
    int tz = threadIdx.x; asm volatile("" : "+v"(tz));
    const int el = tz & 63, efr = el & 15, efq = el >> 4;
#pragma unroll
    for (int ai = 0; ai < 2; ++ai)
#pragma unroll
        for (int m = 0; m < 4; ++m) {
            unsigned pk[2][2];
#pragma unroll
            for (int n = 0; n < 2; ++n) {
                float hv[4];
#pragma unroll
                for (int j = 0; j < 4; ++j) {
                    const float e = __builtin_amdgcn_exp2f(acc[ai][0][m][n][j] * -1.4426950408889634f);
                    hv[j] = acc[ai][1][m][n][j] * __builtin_amdgcn_rcpf(1.0f + e);
                }
                pk[n][0] = pack_bf16(hv[0], hv[1]); pk[n][1] = pack_bf16(hv[2], hv[3]);
            }
            const auto s0 = __builtin_amdgcn_permlane16_swap(pk[0][0], pk[1][0], false, false);
            const auto s1 = __builtin_amdgcn_permlane16_swap(pk[0][1], pk[1][1], false, false);
            const u32x4 w = {s0[0], s1[0], s0[1], s1[1]};
            const unsigned la = (unsigned)(uintptr_t)lds + (ai * 2 + wr) * KB_STAGE + (m * 16 + efr) * 256 + 16 * ((wc * 4 + 2 * (efq & 1) + (efq >> 1)) ^ efr);
            asm volatile("ds_write_b128 %0, %1" :: "v"(la), "v"(w) : "memory");
        }
    WAIT_L(0); BAR();
    const rsrc_t rh = mk_rsrc(P.hbuf, SEQ * FF * 2);
    {
        u32x4 w[8];
#pragma unroll
        for (int i = 0; i < 8; ++i) {
            const int row = 32 * wid + 4 * i + efq;
            RD128(w[i], (unsigned)(uintptr_t)lds + (row >> 6) * KB_STAGE + (row & 63) * 256 + 16 * (efr ^ (row & 15)), 0);
        }
        asm volatile("s_waitcnt lgkmcnt(0)" : "+v"(w[0]), "+v"(w[1]), "+v"(w[2]), "+v"(w[3]), "+v"(w[4]), "+v"(w[5]), "+v"(w[6]), "+v"(w[7]) :: "memory");
#pragma unroll
        for (int i = 0; i < 8; ++i) st16(rh, (unsigned)(((pm * 256 + 32 * wid + 4 * i + efq) * FF + pn * 128 + 8 * efr) * 2), w[i]);
    }
    ST();
#undef LDA
#undef LDB
#undef WAIT_LB
#undef MMA
}

#define KE_S01(XA, XB, YA, YB, ya0, ya1, yb0, yb1, yb2, yb3) do { \
        TRRD(YB[0][0][0], yb0, 0); TRRD(YB[0][0][1], yb0, 1024); RD128(YA[0][0], ya0, 0); TRRD(YB[0][1][0], yb1, 0); TRRD(YB[0][1][1], yb1, 1024); RD128(YA[0][1], ya0, 2048); TRRD(YB[0][2][0], yb2, 0); TRRD(YB[0][2][1], yb2, 1024); RD128(YA[0][2], ya0, 4096); TRRD(YB[0][3][0], yb3, 0); TRRD(YB[0][3][1], yb3, 1024); RD128(YA[0][3], ya0, 6144); TRRD(YB[1][0][0], yb0, 8192); TRRD(YB[1][0][1], yb0, 9216); RD128(YA[1][0], ya1, 0); TRRD(YB[1][1][0], yb1, 8192); TRRD(YB[1][1][1], yb1, 9216); RD128(YA[1][1], ya1, 2048); TRRD(YB[1][2][0], yb2, 8192); TRRD(YB[1][2][1], yb2, 9216); RD128(YA[1][2], ya1, 4096); TRRD(YB[1][3][0], yb3, 8192); TRRD(YB[1][3][1], yb3, 9216); RD128(YA[1][3], ya1, 6144); SCHED(); } while (0)
#define KE_S11(XA, XB, YA, YB, ya0, ya1, yb0, yb1, yb2, yb3) do { \
        KE_M(XA, XB, 0, 0, 0); SCHED(); TRRD(YB[0][0][0], yb0, 0); SCHED(); \
        KE_M(XA, XB, 0, 0, 1); SCHED(); TRRD(YB[0][0][1], yb0, 1024); SCHED(); \
        KE_M(XA, XB, 0, 0, 2); SCHED(); RD128(YA[0][0], ya0, 0); SCHED(); \
        KE_M(XA, XB, 0, 0, 3); SCHED(); TRRD(YB[0][1][0], yb1, 0); SCHED(); \
        KE_M(XA, XB, 0, 1, 0); SCHED(); TRRD(YB[0][1][1], yb1, 1024); SCHED(); \
        KE_M(XA, XB, 0, 1, 1); SCHED(); RD128(YA[0][1], ya0, 2048); SCHED(); \
        KE_M(XA, XB, 0, 1, 2); SCHED(); TRRD(YB[0][2][0], yb2, 0); SCHED(); \
        KE_M(XA, XB, 0, 1, 3); SCHED(); TRRD(YB[0][2][1], yb2, 1024); SCHED(); \
        KE_M(XA, XB, 0, 2, 0); SCHED(); RD128(YA[0][2], ya0, 4096); SCHED(); \
        KE_M(XA, XB, 0, 2, 1); SCHED(); TRRD(YB[0][3][0], yb3, 0); SCHED(); \
        KE_M(XA, XB, 0, 2, 2); SCHED(); TRRD(YB[0][3][1], yb3, 1024); SCHED(); \
        KE_M(XA, XB, 0, 2, 3); SCHED(); RD128(YA[0][3], ya0, 6144); SCHED(); \
        KE_M(XA, XB, 0, 3, 0); SCHED(); TRRD(YB[1][0][0], yb0, 8192); SCHED(); \
        KE_M(XA, XB, 0, 3, 1); SCHED(); TRRD(YB[1][0][1], yb0, 9216); SCHED(); \
        KE_M(XA, XB, 0, 3, 2); SCHED(); RD128(YA[1][0], ya1, 0); SCHED(); \
        KE_M(XA, XB, 0, 3, 3); SCHED(); TRRD(YB[1][1][0], yb1, 8192); SCHED(); \
        KE_M(XA, XB, 1, 0, 0); SCHED(); TRRD(YB[1][1][1], yb1, 9216); SCHED(); \
        KE_M(XA, XB, 1, 0, 1); SCHED(); RD128(YA[1][1], ya1, 2048); SCHED(); \
        KE_M(XA, XB, 1, 0, 2); SCHED(); TRRD(YB[1][2][0], yb2, 8192); SCHED(); \
        KE_M(XA, XB, 1, 0, 3); SCHED(); TRRD(YB[1][2][1], yb2, 9216); SCHED(); \
        KE_M(XA, XB, 1, 1, 0); SCHED(); RD128(YA[1][2], ya1, 4096); SCHED(); \
        KE_M(XA, XB, 1, 1, 1); SCHED(); TRRD(YB[1][3][0], yb3, 8192); SCHED(); \
        KE_M(XA, XB, 1, 1, 2); SCHED(); TRRD(YB[1][3][1], yb3, 9216); SCHED(); \
        KE_M(XA, XB, 1, 1, 3); SCHED(); RD128(YA[1][3], ya1, 6144); SCHED(); \
        KE_M(XA, XB, 1, 2, 0); SCHED(); SCHED(); \
        KE_M(XA, XB, 1, 2, 1); SCHED(); SCHED(); \
        KE_M(XA, XB, 1, 2, 2); SCHED(); SCHED(); \
        KE_M(XA, XB, 1, 2, 3); SCHED(); SCHED(); \
        KE_M(XA, XB, 1, 3, 0); SCHED(); SCHED(); \
        KE_M(XA, XB, 1, 3, 1); SCHED(); SCHED(); \
        KE_M(XA, XB, 1, 3, 2); SCHED(); SCHED(); \
        KE_M(XA, XB, 1, 3, 3); SCHED(); SCHED(); } while (0)
#define KE_S12(XA, XB, YA, YB, ya0, ya1, yb0, yb1, yb2, yb3) do { \
        KE_M(XA, XB, 0, 0, 0); SCHED(); TRRD(YB[0][0][0], yb0, 0); SCHED(); \
        KE_M(XA, XB, 0, 0, 1); SCHED(); TRRD(YB[0][0][1], yb0, 1024); SCHED(); \
        KE_M(XA, XB, 0, 0, 2); SCHED(); RD128(YA[0][0], ya0, 0); SCHED(); \
        KE_M(XA, XB, 0, 0, 3); SCHED(); TRRD(YB[0][1][0], yb1, 0); SCHED(); \
        KE_M(XA, XB, 0, 1, 0); SCHED(); TRRD(YB[0][1][1], yb1, 1024); SCHED(); \
        KE_M(XA, XB, 0, 1, 1); SCHED(); RD128(YA[0][1], ya0, 2048); SCHED(); \
        KE_M(XA, XB, 0, 1, 2); SCHED(); TRRD(YB[0][2][0], yb2, 0); SCHED(); \
        KE_M(XA, XB, 0, 1, 3); SCHED(); TRRD(YB[0][2][1], yb2, 1024); SCHED(); \
        KE_M(XA, XB, 0, 2, 0); SCHED(); TRRD(YB[0][3][0], yb3, 0); SCHED(); \
        KE_M(XA, XB, 0, 2, 1); SCHED(); TRRD(YB[0][3][1], yb3, 1024); SCHED(); \
        KE_M(XA, XB, 0, 2, 2); SCHED(); TRRD(YB[1][0][0], yb0, 8192); SCHED(); \
        KE_M(XA, XB, 0, 2, 3); SCHED(); TRRD(YB[1][0][1], yb0, 9216); SCHED(); \
        KE_M(XA, XB, 0, 3, 0); SCHED(); RD128(YA[1][0], ya1, 0); SCHED(); \
        KE_M(XA, XB, 0, 3, 1); SCHED(); TRRD(YB[1][1][0], yb1, 8192); SCHED(); \
        KE_M(XA, XB, 0, 3, 2); SCHED(); TRRD(YB[1][1][1], yb1, 9216); SCHED(); \
        KE_M(XA, XB, 0, 3, 3); SCHED(); RD128(YA[1][1], ya1, 2048); SCHED(); \
        KE_M(XA, XB, 1, 0, 0); SCHED(); TRRD(YB[1][2][0], yb2, 8192); SCHED(); \
        KE_M(XA, XB, 1, 0, 1); SCHED(); TRRD(YB[1][2][1], yb2, 9216); SCHED(); \
        KE_M(XA, XB, 1, 0, 2); SCHED(); TRRD(YB[1][3][0], yb3, 8192); SCHED(); \
        KE_M(XA, XB, 1, 0, 3); SCHED(); TRRD(YB[1][3][1], yb3, 9216); SCHED(); \
        KE_M(XA, XB, 1, 1, 0); SCHED(); SCHED(); \
        KE_M(XA, XB, 1, 1, 1); SCHED(); SCHED(); \
        KE_M(XA, XB, 1, 1, 2); SCHED(); SCHED(); \
        KE_M(XA, XB, 1, 1, 3); SCHED(); SCHED(); \
        KE_M(XA, XB, 1, 2, 0); SCHED(); SCHED(); \
        KE_M(XA, XB, 1, 2, 1); SCHED(); SCHED(); \
        KE_M(XA, XB, 1, 2, 2); SCHED(); SCHED(); \
        KE_M(XA, XB, 1, 2, 3); SCHED(); SCHED(); \
        KE_M(XA, XB, 1, 3, 0); SCHED(); SCHED(); \
        KE_M(XA, XB, 1, 3, 1); SCHED(); SCHED(); \
        KE_M(XA, XB, 1, 3, 2); SCHED(); SCHED(); \
        KE_M(XA, XB, 1, 3, 3); SCHED(); SCHED(); } while (0)
#define KE_S22(XA, XB, YA, YB, ya0, ya1, yb0, yb1, yb2, yb3) do { \
        KE_M(XA, XB, 0, 0, 0); SCHED(); TRRD(YB[0][0][0], yb0, 0); TRRD(YB[0][0][1], yb0, 1024); SCHED(); \
        KE_M(XA, XB, 0, 0, 1); SCHED(); RD128(YA[0][0], ya0, 0); TRRD(YB[0][1][0], yb1, 0); SCHED(); \
        KE_M(XA, XB, 0, 0, 2); SCHED(); TRRD(YB[0][1][1], yb1, 1024); RD128(YA[0][1], ya0, 2048); SCHED(); \
        KE_M(XA, XB, 0, 0, 3); SCHED(); TRRD(YB[0][2][0], yb2, 0); TRRD(YB[0][2][1], yb2, 1024); SCHED(); \
        KE_M(XA, XB, 0, 1, 0); SCHED(); TRRD(YB[0][3][0], yb3, 0); SCHED(); \
        KE_M(XA, XB, 0, 1, 1); SCHED(); TRRD(YB[0][3][1], yb3, 1024); SCHED(); \
        KE_M(XA, XB, 0, 1, 2); SCHED(); TRRD(YB[1][0][0], yb0, 8192); SCHED(); \
        KE_M(XA, XB, 0, 1, 3); SCHED(); TRRD(YB[1][0][1], yb0, 9216); SCHED(); \
        KE_M(XA, XB, 1, 0, 0); SCHED(); RD128(YA[1][0], ya1, 0); SCHED(); \
        KE_M(XA, XB, 1, 0, 1); SCHED(); TRRD(YB[1][1][0], yb1, 8192); SCHED(); \
        KE_M(XA, XB, 1, 0, 2); SCHED(); TRRD(YB[1][1][1], yb1, 9216); SCHED(); \
        KE_M(XA, XB, 1, 0, 3); SCHED(); RD128(YA[1][1], ya1, 2048); SCHED(); \
        KE_M(XA, XB, 1, 1, 0); SCHED(); TRRD(YB[1][2][0], yb2, 8192); SCHED(); \
        KE_M(XA, XB, 1, 1, 1); SCHED(); TRRD(YB[1][2][1], yb2, 9216); SCHED(); \
        KE_M(XA, XB, 1, 1, 2); SCHED(); TRRD(YB[1][3][0], yb3, 8192); SCHED(); \
        KE_M(XA, XB, 1, 1, 3); SCHED(); TRRD(YB[1][3][1], yb3, 9216); SCHED(); } while (0)
#define KE_S20(XA, XB, YA, YB, ya0, ya1, yb0, yb1, yb2, yb3) do { \
        KE_M(XA, XB, 0, 0, 0); SCHED(); SCHED(); \
        KE_M(XA, XB, 0, 0, 1); SCHED(); SCHED(); \
        KE_M(XA, XB, 0, 0, 2); SCHED(); SCHED(); \
        KE_M(XA, XB, 0, 0, 3); SCHED(); SCHED(); \
        KE_M(XA, XB, 0, 1, 0); SCHED(); SCHED(); \
        KE_M(XA, XB, 0, 1, 1); SCHED(); SCHED(); \
        KE_M(XA, XB, 0, 1, 2); SCHED(); SCHED(); \
        KE_M(XA, XB, 0, 1, 3); SCHED(); SCHED(); \
        KE_M(XA, XB, 1, 0, 0); SCHED(); SCHED(); \
        KE_M(XA, XB, 1, 0, 1); SCHED(); SCHED(); \
        KE_M(XA, XB, 1, 0, 2); SCHED(); SCHED(); \
        KE_M(XA, XB, 1, 0, 3); SCHED(); SCHED(); \
        KE_M(XA, XB, 1, 1, 0); SCHED(); SCHED(); \
        KE_M(XA, XB, 1, 1, 1); SCHED(); SCHED(); \
        KE_M(XA, XB, 1, 1, 2); SCHED(); SCHED(); \
        KE_M(XA, XB, 1, 1, 3); SCHED(); SCHED(); } while (0)

__device__ __forceinline__ void gemm2_loader(const Params& P, LAS unsigned char* lds, const G2Load& g2, int ks, int pm, int pn, int lw, int lane) {
    const int spare = (1 - ks) * 8192;
    u32x4 ey[4], ex[4]; unsigned es;
#define KD_LDY(i) do { const int row = (lw * 4 + (i)) * 8 + (lane >> 3), ck = (lane & 7) ^ (((row >> 1) & 3) << 1); \
        const char* _p = (const char*)P.y2T + ((size_t)(pn * 128 + row) * SEQ + pm * 128 + ks * 64 + 8 * ck) * 2; \
        asm volatile("global_load_dwordx4 %0, %1, off sc1" : "=&v"(ey[i]) : "v"(_p) : "memory"); } while (0)
#define KD_LDX(i) do { const int xr = (lw * 4 + (i)) * 4 + (lane >> 4), xc = (lane & 15) ^ (xr & 15); \
        const char* _p = (const char*)P.x + ((size_t)(pm * 128 + ks * 64 + xr) * DM + pn * 128 + 8 * xc) * 2; \
        asm volatile("global_load_dwordx4 %0, %1, off" : "=&v"(ex[i]) : "v"(_p) : "memory"); } while (0)
#define KD_LDS() do { const char* _p = (const char*)P.stats + (size_t)pm * 128 * 8 + (lw * 64 + lane) * 4; \
        asm volatile("global_load_dword %0, %1, off sc1" : "=&v"(es) : "v"(_p) : "memory"); } while (0)
#define KD_PUT() do { asm volatile("" : "+v"(ey[0]), "+v"(ey[1]), "+v"(ey[2]), "+v"(ey[3]), "+v"(ex[0]), "+v"(ex[1]), "+v"(ex[2]), "+v"(ex[3]), "+v"(es)); \
        _Pragma("unroll") for (int i = 0; i < 4; ++i) { \
            *(LAS u32x4*)(lds + (2 + (lw >> 1)) * KB_STAGE + spare + ((lw & 1) * 4 + i) * 1024 + lane * 16) = ey[i]; \
            *(LAS u32x4*)(lds + (lw >> 1) * KB_STAGE + spare + ((lw & 1) * 4 + i) * 1024 + lane * 16) = ex[i]; } \
        *(LAS unsigned*)(lds + 4 * KB_STAGE + spare + 1024 + (lw * 64 + lane) * 4) = es; } while (0)
        KD_ISSUE1A(0, 0); KD_ISSUE1A(1, 1); KD_ISSUE1A(2, 2); KD_ISSUE1A(3, 3);
        WAIT_V(12); BAR();
        WAIT_V(8); BAR(); KD_ISSUE1(4, 4);
        WAIT_V(12); BAR(); KD_ISSUE1(5, 0);
        WAIT_V(16); BAR(); KD_LDY(0); KD_ISSUE1(6, 1);
        WAIT_V(17); BAR(); KD_ISSUE1(7, 2);
        WAIT_V(17); BAR(); KD_LDX(0); KD_ISSUE1(8, 3);
        WAIT_V(17); BAR(); KD_ISSUE1(9, 4);
        WAIT_V(17); BAR(); KD_LDY(1); KD_ISSUE1(10, 0);
        WAIT_V(17); BAR(); KD_ISSUE1(11, 1);
        WAIT_V(17); BAR(); KD_LDX(1); KD_ISSUE1(12, 2);
        WAIT_V(17); BAR(); KD_ISSUE1(13, 3);
        WAIT_V(17); BAR(); KD_LDY(2); KD_ISSUE1(14, 4);
        WAIT_V(17); BAR(); KD_ISSUE1(15, 0);
        WAIT_V(17); BAR(); KD_LDX(2); KD_ISSUE1(16, 1);
        WAIT_V(17); BAR(); KD_ISSUE1(17, 2);
        WAIT_V(17); BAR(); KD_LDY(3); KD_ISSUE1(18, 3);
        WAIT_V(17); BAR(); KD_ISSUE1(19, 4);
        WAIT_V(17); BAR(); KD_LDX(3); KD_ISSUE1(20, 0);
        WAIT_V(17); BAR(); KD_ISSUE1(21, 1);
        WAIT_V(17); BAR(); KD_LDS(); KD_ISSUE1(22, 2);
        WAIT_V(17); BAR(); KD_ISSUE2(23, 3);
        WAIT_V(15); BAR(); KD_ISSUE2(24, 4);
        WAIT_V(12); BAR(); KD_ISSUE2(25, 0);
        WAIT_V(12); BAR(); KD_ISSUE2(26, 1); BAR();
        WAIT_V(12); BAR(); KD_PUT(); KD_ISSUE2(27, 2); WAIT_L(0);
        WAIT_V(12); BAR(); KD_ISSUE2(28, 3);
        WAIT_V(12); BAR(); KD_ISSUE2(29, 4);
        WAIT_V(12); BAR(); KD_ISSUE2(30, 0);
        WAIT_V(12); BAR(); KD_ISSUE2(31, 1);
        WAIT_V(12); BAR(); KD_ISSUE2(32, 2);
        WAIT_V(12); BAR();
        WAIT_V(6); BAR();
        WAIT_V(0); BAR();
#undef KD_LDY
#undef KD_LDX
#undef KD_LDS
#undef KD_PUT
}
__device__ __forceinline__ void gemm2_mult(const Params& P, LAS unsigned char* lds, int ks, int pm, int pn, int wid, int lane, u64 tag, int& _sti) {
    const int tid = threadIdx.x, spare = (1 - ks) * 8192;
    const int wr = wid >> 1, wc = wid & 1, rg = (wr != ks) ? 1 : 0, fr = lane & 15, fq = lane >> 4, q = (lane & 15) >> 2, p = lane & 3;
    const unsigned ldsb = (unsigned)(uintptr_t)lds;
    const int a1row = wr * 64 + fr, a2row = ks * 64 + rg * 32 + fr;
    const unsigned a1k0 = ldsb + a1row * 128 + 16 * ((0 + fq) ^ (fr & 7)), a1k1 = ldsb + a1row * 128 + 16 * ((4 + fq) ^ (fr & 7));
    const unsigned a2k0 = ldsb + a2row * 128 + 16 * ((0 + fq) ^ (fr & 7)), a2k1 = ldsb + a2row * 128 + 16 * ((4 + fq) ^ (fr & 7));
    unsigned bo[4];
#pragma unroll
    for (int n = 0; n < 4; ++n) bo[n] = ldsb + 16384 + 256 * (8 * fq + q) + 16 * ((8 * wc + 2 * n + (p >> 1)) ^ (4 * q + 2 * (fq & 1))) + 8 * (p & 1);
#define KE_M(Af, Bf, kk, m, n) acc[m][n] = __builtin_amdgcn_mfma_f32_16x16x32_bf16(CAT(Bf[kk][n][0], Bf[kk][n][1]), Af[kk][m], acc[m][n], 0, 0, 0)
#define KE_WAITL(Af, Bf, NM) do { asm volatile("s_waitcnt lgkmcnt(0)" : "+v"(Bf[0][0][0]), "+v"(Bf[0][0][1]), "+v"(Bf[0][1][0]), "+v"(Bf[0][1][1]), "+v"(Bf[0][2][0]), "+v"(Bf[0][2][1]), "+v"(Bf[0][3][0]), "+v"(Bf[0][3][1]), \
        "+v"(Bf[1][0][0]), "+v"(Bf[1][0][1]), "+v"(Bf[1][1][0]), "+v"(Bf[1][1][1]), "+v"(Bf[1][2][0]), "+v"(Bf[1][2][1]), "+v"(Bf[1][3][0]), "+v"(Bf[1][3][1]) :: "memory"); \
        asm volatile("" : "+v"(Af[0][0]), "+v"(Af[0][1]), "+v"(Af[1][0]), "+v"(Af[1][1])); if ((NM) == 4) asm volatile("" : "+v"(Af[0][2]), "+v"(Af[0][3]), "+v"(Af[1][2]), "+v"(Af[1][3])); } while (0)
    f32x4 acc[4][4];
#pragma unroll
    for (int m = 0; m < 4; ++m)
#pragma unroll
        for (int n = 0; n < 4; ++n) acc[m][n] = (f32x4){0.f, 0.f, 0.f, 0.f};
    bf16x8 PA[2][4], QA[2][4]; bf16x4 PB[2][4][2], QB[2][4][2];
    int so = 0;
#define KD_ADDR(AK0, AK1) const unsigned _a0 = (AK0) + so, _a1 = (AK1) + so, _b0 = bo[0] + so, _b1 = bo[1] + so, _b2 = bo[2] + so, _b3 = bo[3] + so
#define KD_NEXT() so = (so == 4 * KB_STAGE) ? 0 : so + KB_STAGE
    BAR();
    ST();
    { KD_ADDR(a1k0, a1k1); KE_S01(QA, QB, PA, PB, _a0, _a1, _b0, _b1, _b2, _b3); KD_NEXT(); }
#define KE_STEP(MAC, NM, AK0, AK1, XA, XB, YA, YB) do { KD_ADDR(AK0, AK1); unsigned _u0 = _a0, _u1 = _a1, _v0 = _b0, _v1 = _b1, _v2 = _b2, _v3 = _b3; \
        asm volatile("" : "+v"(_u0), "+v"(_u1), "+v"(_v0), "+v"(_v1), "+v"(_v2), "+v"(_v3)); \
        KE_WAITL(XA, XB, NM); BAR(); SCHED(); MAC(XA, XB, YA, YB, _u0, _u1, _v0, _v1, _v2, _v3); KD_NEXT(); } while (0)
    const int erow = pm * 128 + ks * 64 + rg * 32 + fr;
    const int ecol = pn * 128 + wc * 64 + 16 * (fq & 1) + 8 * (fq >> 1);
    for (int s = 0; s < 22; s += 2) {
        KE_STEP(KE_S11, 4, a1k0, a1k1, PA, PB, QA, QB);
        KE_STEP(KE_S11, 4, a1k0, a1k1, QA, QB, PA, PB);
    }
    ST();
    KE_STEP(KE_S12, 4, a2k0, a2k1, PA, PB, QA, QB);
    const rsrc_t rp = mk_rsrc(P.part, 256 * 16384);
    LAS unsigned* cnt = (LAS unsigned*)(lds + 4 * KB_STAGE + spare);
    {
        LAS f32x4* ho = (LAS f32x4*)(lds + wc * KB_STAGE + spare);
        if (rg == 0) {
#pragma unroll
            for (int m = 0; m < 2; ++m)
#pragma unroll
                for (int n = 0; n < 4; ++n) ho[(m * 4 + n) * 64 + lane] = acc[2 + m][n];
            if (tid == (ks << 7)) *cnt = 0u;
        } else {
#pragma unroll
            for (int m = 0; m < 4; ++m)
#pragma unroll
                for (int h = 0; h < 2; ++h) {
                    const f32x4 a0 = acc[m][2 * h], a1 = acc[m][2 * h + 1];
                    st16(rp, blockIdx.x * 16384u + (unsigned)(wc * 8192 + (m * 2 + h) * 1024 + lane * 16),
                         (u32x4){pack_bf16(a0[0], a0[1]), pack_bf16(a0[2], a0[3]), pack_bf16(a1[0], a1[1]), pack_bf16(a1[2], a1[3])});
                }
        }
        WAIT_L(0); BAR();
        if (rg == 1) {
#pragma unroll
            for (int m = 0; m < 2; ++m)
#pragma unroll
                for (int n = 0; n < 4; ++n) acc[m][n] = ho[(m * 4 + n) * 64 + lane];
        }
    }
    KE_STEP(KE_S22, 2, a2k0, a2k1, QA, QB, PA, PB);
    KE_STEP(KE_S22, 2, a2k0, a2k1, PA, PB, QA, QB);
    KE_STEP(KE_S22, 2, a2k0, a2k1, QA, QB, PA, PB);
    KE_STEP(KE_S22, 2, a2k0, a2k1, PA, PB, QA, QB);
    if (rg == 1) {
        WAIT_V(0);
        if (lane == 0) {
            const unsigned n = __hip_atomic_fetch_add(cnt, 1u, __ATOMIC_RELAXED, __HIP_MEMORY_SCOPE_WORKGROUP);
            if (n == 1u) signal(P.sync + SY_P + blockIdx.x * 16, tag);
        }
    }
    KE_STEP(KE_S22, 2, a2k0, a2k1, QA, QB, PA, PB);
    KE_STEP(KE_S22, 2, a2k0, a2k1, PA, PB, QA, QB);
    KE_STEP(KE_S22, 2, a2k0, a2k1, QA, QB, PA, PB);
    u64 pf;
    {
        const u64* pfl = P.sync + SY_P + (blockIdx.x ^ 1) * 16;
        asm volatile("global_load_dwordx2 %0, %1, off sc1" : "=&v"(pf) : "v"(pfl) : "memory");
    }
    KE_STEP(KE_S22, 2, a2k0, a2k1, PA, PB, QA, QB);
    KE_STEP(KE_S22, 2, a2k0, a2k1, QA, QB, PA, PB);
    ST();
    asm volatile("s_waitcnt vmcnt(0)" : "+v"(pf) :: "memory");
    if (!__all(pf == tag)) poll_words(P.sync + SY_P + (blockIdx.x ^ 1) * 16, true, tag);
    asm volatile("" ::: "memory");
    u32x4 pin[2][2];
#pragma unroll
    for (int m = 0; m < 2; ++m)
#pragma unroll
        for (int h = 0; h < 2; ++h) {
            const unsigned o = (blockIdx.x ^ 1) * 16384u + (unsigned)(wc * 8192 + ((2 * rg + m) * 2 + h) * 1024 + lane * 16);
            asm volatile("buffer_load_dwordx4 %0, %1, %2, 0 offen sc1" : "=&v"(pin[m][h]) : "v"(o), "s"(rp) : "memory");
        }
    KE_WAITL(PA, PB, 2); SCHED();
    KE_S20(PA, PB, QA, QB, 0, 0, 0, 0, 0, 0);
    ST();
    bf16x4 yv[2][2][2]; u32x4 xv[2][2]; f32x2 st[2];
#pragma unroll
    for (int m = 0; m < 2; ++m) {
        st[m] = *(const LAS f32x2*)(lds + 4 * KB_STAGE + spare + 1024 + (ks * 64 + rg * 32 + 16 * m + fr) * 8);
#pragma unroll
        for (int h = 0; h < 2; ++h) {
            const int xr = 16 * m + fr, xc = 8 * wc + 4 * h + 2 * (fq & 1) + (fq >> 1);
            xv[m][h] = *(const LAS u32x4*)(lds + rg * KB_STAGE + spare + xr * 256 + 16 * (xc ^ (xr & 15)));
        }
    }
#pragma unroll
    for (int m = 0; m < 2; ++m)
#pragma unroll
        for (int h = 0; h < 2; ++h) {
            const int crow = 32 * h + 16 * (fq & 1) + 8 * (fq >> 1) + q;
            const int tck = (rg * 32 + 16 * m) / 8 + (p >> 1);
            const unsigned yb = ldsb + (2 + wc) * KB_STAGE + spare + crow * 128 + 8 * (p & 1);
            TRRD(yv[m][h][0], yb + 16 * (tck ^ (((crow >> 1) & 3) << 1)), 0);
            TRRD(yv[m][h][1], yb + 16 * (tck ^ ((((crow + 4) >> 1) & 3) << 1)), 512);
        }
    asm volatile("s_waitcnt vmcnt(0) lgkmcnt(0)" : "+v"(pin[0][0]), "+v"(pin[0][1]), "+v"(pin[1][0]), "+v"(pin[1][1]), "+v"(yv[0][0][0]), "+v"(yv[0][0][1]), "+v"(yv[0][1][0]), "+v"(yv[0][1][1]),
                 "+v"(yv[1][0][0]), "+v"(yv[1][0][1]), "+v"(yv[1][1][0]), "+v"(yv[1][1][1]) :: "memory");
    const rsrc_t ro = mk_rsrc(P.out, SEQ * DM * 2);
#pragma unroll
    for (int m = 0; m < 2; ++m) {
        const float rs = st[m][1], mrs = -st[m][0] * rs;
        u32x4 res[2];
#pragma unroll
        for (int h = 0; h < 2; ++h) {
            const u32x4 w = pin[m][h];
            const f32x4 a0 = acc[m][2 * h] + (f32x4){bf_lo(w[0]), bf_hi(w[0]), bf_lo(w[1]), bf_hi(w[1])};
            const f32x4 a1 = acc[m][2 * h + 1] + (f32x4){bf_lo(w[2]), bf_hi(w[2]), bf_lo(w[3]), bf_hi(w[3])};
            float o[8];
#pragma unroll
            for (int j = 0; j < 4; ++j) {
                const auto sw = __builtin_amdgcn_permlane16_swap(__float_as_uint(a0[j]), __float_as_uint(a1[j]), false, false);
                o[j] = __uint_as_float(sw[0]); o[4 + j] = __uint_as_float(sw[1]);
            }
#pragma unroll
            for (int j = 0; j < 8; ++j) {
                const unsigned xw = xv[m][h][j >> 1];
                const float xf = (j & 1) ? bf_hi(xw) : bf_lo(xw);
                const float yf = __uint_as_float((unsigned)(u16)yv[m][h][j >> 2][j & 3] << 16);
                o[j] += xf + (xf * rs + mrs) * yf;
            }
            res[h] = (u32x4){pack_bf16(o[0], o[1]), pack_bf16(o[2], o[3]), pack_bf16(o[4], o[5]), pack_bf16(o[6], o[7])};
        }
        u32x4 sa, sb;
#pragma unroll
        for (int i = 0; i < 4; ++i) {
            const unsigned y = (unsigned)__builtin_amdgcn_update_dpp(0, (int)res[1][i], 0x128  , 0xf, 0xf, false);
            sa[i] = fr < 8 ? res[0][i] : y; sb[i] = fr < 8 ? y : res[0][i];
        }
        const int rbase = erow - fr + 16 * m + (fr & 7), cbase = ecol;
        st16(ro, (unsigned)((rbase * DM + cbase + (fr < 8 ? 0 : 32)) * 2), sa);
        st16(ro, (unsigned)(((rbase + 8) * DM + cbase + (fr < 8 ? 32 : 0)) * 2), sb);
    }
    ST();
#undef KE_M
#undef KE_WAITL
#undef KE_STEP
#undef KD_ADDR
#undef KD_NEXT
}

constexpr int PA_K = 0, PA_V = 17408, PA_T = 34816, PA_C = 51200;
__global__ __launch_bounds__(512, 2) void fused_kernel(Params P) {
    extern __shared__ __attribute__((aligned(16))) unsigned char shm[];
    LAS unsigned char* lds = (LAS unsigned char*)shm;
    int _sti = 0;
    ST();
    const int tid = threadIdx.x, wid = __builtin_amdgcn_readfirstlane(tid >> 6), lane = tid & 63;
    const int b = blockIdx.x, xcd = b & 7, bi = b >> 3;
    const u64 tag = __hip_atomic_load((gu64*)(P.sync + SY_EPOCH), __ATOMIC_RELAXED, __HIP_MEMORY_SCOPE_AGENT) + 1ull;
    const bool is_gemm = bi < 28;
    const int pm1 = 4 * (xcd & 1) + (bi & 3), pn1 = 7 * (xcd >> 1) + (bi >> 2), cb = xcd * 4 + (bi - 28);
    const G1Addr ga = gemm1_addr(P, pm1, is_gemm ? pn1 : 0);
    ConvPre cpre;
    {
        const int row = b * 8 + wid, cg = b & 15, rt = b >> 4;
        const u16* xr = P.x + (size_t)row * DM;
        u32x4 a, bq; u32x2 wt;
        asm volatile("global_load_dwordx4 %0, %1, off" X_POL : "=&v"(a) : "v"(xr + lane * 8) : "memory");
        asm volatile("global_load_dwordx4 %0, %1, off offset:1024" X_POL : "=&v"(bq) : "v"(xr + lane * 8) : "memory");
        asm volatile("global_load_dwordx2 %0, %1, off" : "=&v"(wt) : "v"(P.shk + (size_t)(cg * 64 + lane) * 4) : "memory");
#pragma unroll
        for (int i = 0; i < 2; ++i) {
            const int j = wid + 8 * i, kr = 128 * rt - 8 + 8 * j + (lane >> 3);
            __builtin_amdgcn_global_load_lds((const GAS unsigned*)(P.kin + (size_t)(kr < 0 ? 0 : kr) * DM + cg * 64 + (lane & 7) * 8), (LAS unsigned*)(lds + PA_K + j * 1024), 16, 0, 2);
            __builtin_amdgcn_global_load_lds((const GAS unsigned*)(P.vin + (size_t)(128 * rt + 8 * j + (lane >> 3)) * DM + cg * 64 + (lane & 7) * 8), (LAS unsigned*)(lds + PA_V + j * 1024), 16, 0, 2);
        }
        if (wid == 0) __builtin_amdgcn_global_load_lds((const GAS unsigned*)(P.kin + (size_t)(128 * rt + 120 + (lane >> 3)) * DM + cg * 64 + (lane & 7) * 8), (LAS unsigned*)(lds + PA_K + 16 * 1024), 16, 0, 2);
        WAIT_V(4);
        asm volatile("" : "+v"(a), "+v"(bq), "+v"(wt));
        float v[16];
#pragma unroll
        for (int i = 0; i < 4; ++i) { v[2 * i] = bf_lo(a[i]); v[2 * i + 1] = bf_hi(a[i]); v[8 + 2 * i] = bf_lo(bq[i]); v[9 + 2 * i] = bf_hi(bq[i]); }
        float s = 0.f;
#pragma unroll
        for (int i = 0; i < 16; ++i) s += v[i];
        const float mean = wave_sum(s) * (1.0f / DM);
        float q = 0.f;
#pragma unroll
        for (int i = 0; i < 16; ++i) { const float d = v[i] - mean; q += d * d; }
        const float mb = bf_round(mean), vb = bf_round(wave_sum(q) * (1.0f / DM));
        const float sb = bf_round(sqrtf(bf_round(vb + 1e-5f)));
        const float rs = 1.0f / sb;
        u32x4 oa, ob;
#pragma unroll
        for (int i = 0; i < 4; ++i) {
            oa[i] = pack_bf16(bf_round(v[2 * i] - mb) * rs, bf_round(v[2 * i + 1] - mb) * rs);
            ob[i] = pack_bf16(bf_round(v[8 + 2 * i] - mb) * rs, bf_round(v[9 + 2 * i] - mb) * rs);
        }
        const float w0 = bf_lo(wt[0]), w1 = bf_hi(wt[0]), w2 = bf_lo(wt[1]), w3 = bf_hi(wt[1]);
        ST();
        const rsrc_t rx = mk_rsrc(P.xn, SEQ * DM * 2);
        st16(rx, (unsigned)(row * DM * 2 + lane * 16), oa); st16(rx, (unsigned)(row * DM * 2 + 1024 + lane * 16), ob);
        st8(mk_rsrc(P.stats, SEQ * 8), lane == 0 ? (unsigned)(row * 8) : 0xfffffff0u, (u32x2){__float_as_uint(mb), __float_as_uint(rs)});
        if (!is_gemm) { conv_prefetch(P, cb, cpre); WAIT_V(4); } else { WAIT_V(0); }
        WAIT_L(0); BAR();
        if (tid == 0) signal(P.sync + SY_X + b, tag);
        if (is_gemm) { STAGE_B(SB(0, 0), ga.cG, 0); STAGE_B(SB(0, 1), ga.cU, 0); STAGE_B(SB(1, 0), ga.cG, 1); STAGE_B(SB(1, 1), ga.cU, 1); }
        if (tid == 0) *(LAS unsigned*)(lds + PA_C + 4) = 0u;
        ST();
        float kf[19], kv[16];
#pragma unroll
        for (int i = 0; i < 19; ++i) kf[i] = bf_lo((unsigned)*(const LAS u16*)(lds + PA_K + (16 * wid + 5 + i) * 128 + lane * 2));
        if (rt == 0 && wid == 0) { kf[0] = 0.f; kf[1] = 0.f; kf[2] = 0.f; }
#pragma unroll
        for (int i = 0; i < 16; ++i) {
            const float vf = bf_lo((unsigned)*(const LAS u16*)(lds + PA_V + (16 * wid + i) * 128 + lane * 2));
            kv[i] = (kf[i] * w3 + kf[i + 1] * w2 + kf[i + 2] * w1 + kf[i + 3] * w0) * vf;
        }
#pragma unroll
        for (int j = 0; j < 2; ++j) {
            u32x4 o;
#pragma unroll
            for (int i = 0; i < 4; ++i) o[i] = pack_bf16(kv[8 * j + 7 - 2 * i], kv[8 * j + 6 - 2 * i]);
            *(LAS u32x4*)(lds + PA_T + lane * 256 + 16 * ((2 * wid + j) ^ (lane & 15))) = o;
        }
        ST();
        WAIT_L(0); BAR();
        u32x4 kt[2];
#pragma unroll
        for (int i = 0; i < 2; ++i) { const int ch = 8 * wid + 4 * i + (lane >> 4), ck = lane & 15; kt[i] = *(const LAS u32x4*)(lds + PA_T + ch * 256 + 16 * (ck ^ (ch & 15))); }
        const rsrc_t rk = mk_rsrc(P.kvT, SEQ * DM * 2);
#define PA_PUTK() do { _Pragma("unroll") for (int i = 0; i < 2; ++i) st16(rk, (unsigned)(((cg * 64 + 8 * wid + 4 * i + (lane >> 4)) * SEQ + 128 * rt + 8 * (lane & 15)) * 2), kt[i]); } while (0)
        if (is_gemm) {
            if (wid != 0) PA_PUTK();
            if (wid == 0) { poll_words(P.sync + SY_X + pm1 * 32 + (lane & 31), true, tag); PA_PUTK(); }
            WAIT_L(0); BAR();
            STAGE_A(SA(0, 0), 0, 0); STAGE_A(SA(0, 1), 1, 0); STAGE_A(SA(1, 0), 0, 1);
            WAIT_V(6);
        } else {
            PA_PUTK();
            WAIT_V(0);
        }
        if (last_wave((LAS unsigned*)(lds + PA_C + 4), 8) && lane == 0) signal(P.sync + SY_K + (b & 15) * 16 + (b >> 4), tag);
#undef PA_PUTK
        ST();
    }
    ST();
    const int ks = xcd & 1, pm2 = 8 * ((xcd >> 1) & 1) + (bi & 7), pn2 = 4 * (xcd >> 2) + (bi >> 3);
    const G2Load g2 = gemm2_addr(P, ks, pm2, pn2, wid & 3, lane);
    if (is_gemm) {
        ST();
        gemm1_block(P, lds, ga, g2, ks, pm1, pn1, _sti);
    } else {
        if (wid == 0) poll_words(P.sync + SY_K + (cb >> 1) * 16 + (lane & 15), true, tag);
        BAR();
        ST();
        ST();
        asm volatile("" : "+v"(cpre.nh[0]), "+v"(cpre.nh[1]), "+v"(cpre.nh[2]), "+v"(cpre.nh[3]));
        conv_block(P, lds, cb, cpre, _sti);
        WAIT_L(0); BAR();
        if (tid == 0) *(LAS unsigned*)(lds + SYNC_CNT) = 0u;
        WAIT_L(0); BAR();
        if (wid >= 4) { const int lw = wid - 4; KD_ISSUE1B(0, 0); KD_ISSUE1B(1, 1); KD_ISSUE1B(2, 2); KD_ISSUE1B(3, 3); }
        ST();
    }
    if (wid >= 4 && !is_gemm) { WAIT_V(16); } else { WAIT_V(0); }
    if (last_wave((LAS unsigned*)(lds + SYNC_CNT), 8) && lane == 0) signal(is_gemm ? P.sync + SY_H + pm1 * 32 + pn1 : P.sync + SY_Y + cb, tag);
    if (wid == 4) poll_words(lane < 28 ? P.sync + SY_H + (pm2 >> 1) * 32 + lane : P.sync + SY_Y + 4 * pn2 + (lane & 3), lane < 32, tag);
    WAIT_L(0); BAR();
    ST();
    if (wid >= 4) {
        gemm2_loader(P, lds, g2, ks, pm2, pn2, wid - 4, lane);
        if (b == 0 && wid == 4) {
#pragma unroll
            for (int i = 0; i < 4; ++i) poll_words(P.sync + SY_X + i * 64 + lane, true, tag);
            if (lane == 0) signal(P.sync + SY_EPOCH, tag);
        }
    } else {
        gemm2_mult(P, lds, ks, pm2, pn2, wid, lane, tag, _sti);
    }
}

extern "C" void kernel_launch(void* const* d_in, const int* in_sizes, int n_in, void* d_out, int out_size, void* d_ws, size_t ws_size, hipStream_t stream) {
    unsigned char* ws = (unsigned char*)d_ws;
    const size_t MB = 1u << 20;
    Params P;
    P.x = (const u16*)d_in[0]; P.kin = (const u16*)d_in[1]; P.vin = (const u16*)d_in[2]; P.shk = (const u16*)d_in[3]; P.ssmk = (const u16*)d_in[4]; P.ssmD = (const u16*)d_in[5];
    P.wg = (const u16*)d_in[7]; P.wu = (const u16*)d_in[8]; P.wd = (const u16*)d_in[9];
    P.out = (u16*)d_out;
    P.xn = (u16*)(ws + 0 * MB); P.kvT = (u16*)(ws + 4 * MB); P.y2T = (u16*)(ws + 8 * MB); P.hbuf = (u16*)(ws + 16 * MB);
    P.stats = (f32x2*)(ws + 12 * MB); P.part = (float*)(ws + 32 * MB); P.sync = (u64*)(ws + 13 * MB); P.stamps = (u64*)(ws + 64 * MB);
    (void)hipFuncSetAttribute((const void*)fused_kernel, hipFuncAttributeMaxDynamicSharedMemorySize, LDS_BYTES);
    fused_kernel<<<256, 512, LDS_BYTES, stream>>>(P);
}
```

```cpp
#include <hip/hip_runtime.h>
#include <hip/hip_bf16.h>
#include <stdint.h>

#define LAS __attribute__((address_space(3)))
#define GAS __attribute__((address_space(1)))
typedef unsigned short u16;
typedef short bf16x8 __attribute__((ext_vector_type(8)));
typedef short bf16x4 __attribute__((ext_vector_type(4)));
typedef float f32x4 __attribute__((ext_vector_type(4)));
typedef float f32x2 __attribute__((ext_vector_type(2)));
typedef unsigned u32x4 __attribute__((ext_vector_type(4)));
typedef unsigned u32x2 __attribute__((ext_vector_type(2)));
typedef __bf16 bf16x2_t __attribute__((ext_vector_type(2)));
typedef unsigned long long u64;
typedef GAS u64 gu64;

constexpr int SEQ = 2048, DM = 1024, FF = 3584;

__device__ __forceinline__ unsigned pack_bf16(float lo, float hi) {
    f32x2 v = {lo, hi};
    return __builtin_bit_cast(unsigned, __builtin_convertvector(v, bf16x2_t));
}
__device__ __forceinline__ float bf_lo(unsigned u) { return __uint_as_float(u << 16); }
__device__ __forceinline__ float bf_hi(unsigned u) { return __uint_as_float(u & 0xffff0000u); }
__device__ __forceinline__ float bf_round(float f) { return bf_lo(pack_bf16(f, f)); }

__device__ __forceinline__ float wave_sum(float v) {
    v += __builtin_bit_cast(float, __builtin_amdgcn_update_dpp(0, __builtin_bit_cast(int, v), 0x111, 0xf, 0xf, true));
    v += __builtin_bit_cast(float, __builtin_amdgcn_update_dpp(0, __builtin_bit_cast(int, v), 0x112, 0xf, 0xf, true));
    v += __builtin_bit_cast(float, __builtin_amdgcn_update_dpp(0, __builtin_bit_cast(int, v), 0x114, 0xf, 0xf, true));
    v += __builtin_bit_cast(float, __builtin_amdgcn_update_dpp(0, __builtin_bit_cast(int, v), 0x118, 0xf, 0xf, true));
    v += __builtin_bit_cast(float, __builtin_amdgcn_update_dpp(0, __builtin_bit_cast(int, v), 0x142, 0xa, 0xf, true));
    v += __builtin_bit_cast(float, __builtin_amdgcn_update_dpp(0, __builtin_bit_cast(int, v), 0x143, 0xc, 0xf, true));
    return __builtin_bit_cast(float, __builtin_amdgcn_readlane(__builtin_bit_cast(int, v), 63));
}
typedef __amdgpu_buffer_rsrc_t rsrc_t;
__device__ __forceinline__ rsrc_t mk_rsrc(const void* base, unsigned bytes) { return __builtin_amdgcn_make_buffer_rsrc((void*)base, 0, bytes, 0x00020000); }
__device__ __forceinline__ void st16(rsrc_t r, unsigned off, u32x4 v) { __builtin_amdgcn_raw_buffer_store_b128(v, r, off, 0, 16); }
__device__ __forceinline__ void st8(rsrc_t r, unsigned off, u32x2 v) { __builtin_amdgcn_raw_buffer_store_b64(v, r, off, 0, 16); }
__device__ __forceinline__ u32x4 ld16_stream(const void* p) { return __builtin_nontemporal_load((const u32x4*)p); }

#define WAIT_V(n) asm volatile("s_waitcnt vmcnt(" #n ")" ::: "memory")
#define WAIT_L(n) asm volatile("s_waitcnt lgkmcnt(" #n ")" ::: "memory")
#define BAR() __builtin_amdgcn_s_barrier()
#define SCHED() __builtin_amdgcn_sched_barrier(0)
#define GLDS(gptr, ldsptr) __builtin_amdgcn_global_load_lds((const GAS unsigned*)(gptr), (LAS unsigned*)(ldsptr), 16, 0, 0)
#define DEV_AUX 16
#define GLDS_DEV(gptr, ldsptr) __builtin_amdgcn_global_load_lds((const GAS unsigned*)(gptr), (LAS unsigned*)(ldsptr), 16, 0, DEV_AUX)
#define TRRD(dst, addr, off) asm volatile("ds_read_b64_tr_b16 %0, %1 offset:%c2" : "=&v"(dst) : "v"(addr), "i"(off) : "memory")
#define RD128(dst, addr, off) asm volatile("ds_read_b128 %0, %1 offset:%c2" : "=&v"(dst) : "v"(addr), "i"(off) : "memory")
#define CAT(lo, hi) __builtin_shufflevector(lo, hi, 0, 1, 2, 3, 4, 5, 6, 7)

#define X_POL ""
#define ST()

struct Params {
    const u16 *x, *kin, *vin, *shk, *ssmk, *ssmD, *wg, *wu, *wd;
    u16 *out, *xn, *kvT, *y2T, *hbuf;
    f32x2* stats;
    float* part;
    u64* sync;
    u64* stamps;
};
constexpr int SY_EPOCH = 0, SY_X = 128, SY_K = 512, SY_H = 1024, SY_Y = 1536, SY_P = 2048;

__device__ __forceinline__ void signal(u64* word, u64 tag) { __hip_atomic_store((gu64*)word, tag, __ATOMIC_RELAXED, __HIP_MEMORY_SCOPE_AGENT); }
__device__ __forceinline__ void poll_words(const u64* word, bool want, u64 tag) {
    const u64 t0 = __builtin_amdgcn_s_memrealtime();
    for (;;) {
        const u64 v = want ? __hip_atomic_load((gu64*)word, __ATOMIC_RELAXED, __HIP_MEMORY_SCOPE_AGENT) : tag;
        if (__all(v == tag)) break;
        if (__builtin_amdgcn_s_memrealtime() - t0 > 5000000ull) break;
        __builtin_amdgcn_s_sleep(2);
    }
}
__device__ __forceinline__ bool last_wave(LAS unsigned* cnt, unsigned n) {
    unsigned old = 0; unsigned long long ex; const unsigned addr = (unsigned)(uintptr_t)cnt, one = 1u;
    asm volatile("s_mov_b64 %1, exec\n\ts_mov_b64 exec, 1\n\tds_add_rtn_u32 %0, %2, %3\n\ts_waitcnt lgkmcnt(0)\n\ts_mov_b64 exec, %1" : "+v"(old), "=&s"(ex) : "v"(addr), "v"(one) : "memory");
    return (unsigned)__builtin_amdgcn_readfirstlane(old) == n - 1;
}

constexpr int CV_XB0 = 512, CV_XB1 = 4608 + 512, CV_HE = 9216, CV_HO = 9216 + 4160, CV_WAVE = 20480;
struct ConvPre { u32x4 nh[4]; };
__device__ __forceinline__ void conv_prefetch(const Params& P, int cb, ConvPre& pre) {
    const int wid = __builtin_amdgcn_readfirstlane(threadIdx.x >> 6), lane = threadIdx.x & 63;
#pragma unroll
    for (int i = 0; i < 4; ++i) { const u16* q = P.ssmk + (size_t)(cb * 32 + wid * 4) * SEQ + (i * 64 + lane) * 8; asm volatile("global_load_dwordx4 %0, %1, off nt" : "=&v"(pre.nh[i]) : "v"(q) : "memory"); }
}
__device__ __forceinline__ void conv_block(const Params& P, LAS unsigned char* lds, int cb, ConvPre& pre, int& _sti) {
    const int tid = threadIdx.x, c0 = cb * 32;
    const int wid = __builtin_amdgcn_readfirstlane(tid >> 6), lane = tid & 63;
    LAS unsigned char* wl = lds + wid * CV_WAVE;
    const int chw = c0 + wid * 4;
    const rsrc_t rkv = mk_rsrc(P.kvT, SEQ * DM * 2); const u16* ssmk = P.ssmk;
    u32x4 nx[4], nh[4], no[4];
#define CV_GX(ch) do { _Pragma("unroll") for (int i = 0; i < 4; ++i) nx[i] = __builtin_amdgcn_raw_buffer_load_b128(rkv, (unsigned)(((ch) * SEQ + (i * 64 + lane) * 8) * 2), 0, 16); } while (0)
#define CV_GH(ch) do { _Pragma("unroll") for (int i = 0; i < 4; ++i) nh[i] = ld16_stream(ssmk + (size_t)(ch) * SEQ + (i * 64 + lane) * 8); } while (0)
    CV_GX(chw);
    if (lane < 32) { *(LAS u32x4*)(wl + lane * 16) = (u32x4){0u, 0u, 0u, 0u}; *(LAS u32x4*)(wl + 4608 + lane * 16) = (u32x4){0u, 0u, 0u, 0u}; }
    if (lane < 2) { *(LAS u32x4*)(wl + CV_HE + lane * 16) = (u32x4){0u, 0u, 0u, 0u}; *(LAS u32x4*)(wl + CV_HO + lane * 16) = (u32x4){0u, 0u, 0u, 0u}; }
    const int r = lane & 15, g = lane >> 4, g0 = g & 1, gh = g >> 1;
    const int s0 = r - 8 * g0 + 9 + 16 * gh;
    const int aoff = (r & 1) ? (CV_HE + 2 * s0) : (CV_HO + 2 * (s0 + 1));
    const int boff = 32 * (r - gh) + 16 * g0 - 448;
    const rsrc_t ry = mk_rsrc(P.y2T, SEQ * DM * 2);
    LAS unsigned char* abase[4];
#pragma unroll
    for (int i = 0; i < 4; ++i) { unsigned a = (unsigned)(uintptr_t)(wl + aoff + 1024 * i); asm volatile("" : "+v"(a)); abase[i] = (LAS unsigned char*)(uintptr_t)a; }
    unsigned dmask[4];
#pragma unroll
    for (int i = 0; i < 4; ++i) dmask[i] = (gh == 0 && 8 * g0 + 7 - 2 * i == r) ? 0x0000ffffu : (gh == 0 && 8 * g0 + 6 - 2 * i == r) ? 0xffff0000u : 0u;
    const u32x2 dpair = *(const u32x2*)(P.ssmD + chw);
#define CV_PUTX(par) do { _Pragma("unroll") for (int i = 0; i < 4; ++i) *(LAS u32x4*)(wl + ((par) ? CV_XB1 : CV_XB0) + (i * 64 + lane) * 16) = nx[i]; } while (0)
#define CV_SHIFT() do { unsigned rot[4]; \
        _Pragma("unroll") for (int i = 0; i < 4; ++i) rot[i] = (unsigned)__builtin_amdgcn_update_dpp(0, (int)nh[i][3], 0x13C  , 0xf, 0xf, false); \
        _Pragma("unroll") for (int i = 0; i < 4; ++i) { \
            const unsigned prev = (lane == 0) ? (i == 0 ? 0u : rot[i > 0 ? i - 1 : 0]) : rot[i]; \
            no[i][0] = __builtin_amdgcn_alignbit(nh[i][0], prev, 16); no[i][1] = __builtin_amdgcn_alignbit(nh[i][1], nh[i][0], 16); \
            no[i][2] = __builtin_amdgcn_alignbit(nh[i][2], nh[i][1], 16); no[i][3] = __builtin_amdgcn_alignbit(nh[i][3], nh[i][2], 16); } } while (0)
#define CV_PUTH() do { _Pragma("unroll") for (int i = 0; i < 4; ++i) { \
            *(LAS u32x4*)(wl + CV_HE + 32 + (i * 64 + lane) * 16) = nh[i]; *(LAS u32x4*)(wl + CV_HO + 32 + (i * 64 + lane) * 16) = no[i]; } } while (0)
#define CV_LOAD(buf, par, v) do { _Pragma("unroll") for (int u = 0; u < 8; ++u) { \
            bf[buf][u] = *(const LAS bf16x8*)(wl + ((par) ? CV_XB1 : CV_XB0) + boff + 512 * u + 64 * (7 - (v))); \
            const LAS unsigned* pa = (const LAS unsigned*)(abase[u >> 1] + 64 * (8 * (u & 1) + (v))); \
            u32x4 t; t[0] = pa[0]; t[1] = pa[1]; t[2] = pa[2]; t[3] = pa[3]; \
            af[buf][u] = __builtin_bit_cast(bf16x8, t); } } while (0)
#define CV_EPI(a, ch, c) do { u32x2 o; o[0] = pack_bf16(acc[a][c][0], acc[a][c][1]); o[1] = pack_bf16(acc[a][c][2], acc[a][c][3]); \
            st8(ry, (unsigned)(((ch) * SEQ + 256 * (c) + 16 * r + 4 * g) * 2), o); } while (0)
    bf16x8 af[2][8], bf[2][8];
    f32x4 acc[2][8];
    unsigned touch = 0;
#pragma unroll
    for (int i = 0; i < 4; ++i) nh[i] = pre.nh[i];
    CV_SHIFT(); CV_PUTH();
    CV_PUTX(0);
    CV_LOAD(0, 0, 0);
    CV_GX(chw + 1); CV_GH(chw + 1);
    SCHED();
#pragma unroll
    for (int cc = 0; cc < 4; ++cc) {
        const int a = cc & 1;
        if (cc == 0) __builtin_amdgcn_s_setprio(3); else if (cc == 1) __builtin_amdgcn_s_setprio(2); else if (cc == 2) __builtin_amdgcn_s_setprio(1); else __builtin_amdgcn_s_setprio(0);
        const unsigned dw = dpair[cc >> 1], dd = (cc & 1) ? ((dw & 0xffff0000u) | (dw >> 16)) : ((dw << 16) | (dw & 0xffffu));
        const u32x4 adw = {dd & dmask[0], dd & dmask[1], dd & dmask[2], dd & dmask[3]};
        const bf16x8 ad = __builtin_bit_cast(bf16x8, adw);
#pragma unroll
        for (int v = 0; v < 8; ++v) {
            const int cur = v & 1, nxt = cur ^ 1;
            if (v < 7) CV_LOAD(nxt, a, v + 1);
            else if (cc < 3) { CV_PUTH(); CV_LOAD(nxt, a ^ 1, 0); }
            if (v == 4 && cc < 3) CV_PUTX(a ^ 1);
            if (v == 5 && cc < 3) { CV_SHIFT(); if (cc < 2) CV_GX(chw + cc + 2); }
            if (v == 0 && cc > 0 && cc < 3) CV_GH(chw + cc + 1);
            if (v == 3) {
                const int ln = ((cb * 8 + wid) * 4 + cc) * 64 + lane;
                const char* q = (const char*)P.wd + (size_t)(ln < FF * 16 ? ln : 0) * 128;
                asm volatile("global_load_dword %0, %1, off" : "+v"(touch) : "v"(q) : "memory");
            }
            if (v == 0) {
#pragma unroll
                for (int c = 0; c < 8; ++c) acc[a][c] = __builtin_amdgcn_mfma_f32_16x16x32_bf16(ad, bf[cur][c], (f32x4){0.f, 0.f, 0.f, 0.f}, 0, 0, 0);
            }
#pragma unroll
            for (int u = 0; u < 8; ++u)
#pragma unroll
                for (int c = u; c < 8; ++c) acc[a][c] = __builtin_amdgcn_mfma_f32_16x16x32_bf16(af[cur][u], bf[cur][c - u], acc[a][c], 0, 0, 0);
            if (cc > 0 && v >= 1 && v < 3) {
#pragma unroll
                for (int c = 0; c < 4; ++c) CV_EPI(a ^ 1, chw + cc - 1, 4 * (v - 1) + c);
            }
            if (v == 7) {
                if (cc < 3) {
#pragma unroll
                    for (int i = 0; i < 8; ++i) { __builtin_amdgcn_sched_group_barrier(0x008, 1, 0); __builtin_amdgcn_sched_group_barrier(0x200, 1, 0); }
#pragma unroll
                    for (int i = 0; i < 24; ++i) { __builtin_amdgcn_sched_group_barrier(0x008, 1, 0); __builtin_amdgcn_sched_group_barrier(0x100, 1, 0); }
                }
            } else {
#pragma unroll
                for (int i = 0; i < 24; ++i) { __builtin_amdgcn_sched_group_barrier(0x008, 1, 0); __builtin_amdgcn_sched_group_barrier(0x100, 1, 0); }
                if (v == 0 && cc > 0 && cc < 3) {
#pragma unroll
                    for (int i = 0; i < 4; ++i) { __builtin_amdgcn_sched_group_barrier(0x008, 1, 0); __builtin_amdgcn_sched_group_barrier(0x020, 1, 0); }
                }
                if (cc > 0 && v >= 1 && v < 3) {
#pragma unroll
                    for (int i = 0; i < 4; ++i) { __builtin_amdgcn_sched_group_barrier(0x008, 1, 0); __builtin_amdgcn_sched_group_barrier(0x002, 2, 0); __builtin_amdgcn_sched_group_barrier(0x008, 1, 0); __builtin_amdgcn_sched_group_barrier(0x040, 1, 0); }
                }
                if (v == 4 && cc < 3) {
#pragma unroll
                    for (int i = 0; i < 4; ++i) { __builtin_amdgcn_sched_group_barrier(0x008, 1, 0); __builtin_amdgcn_sched_group_barrier(0x200, 1, 0); }
                }
                if (v == 5 && cc < 3) {
#pragma unroll
                    for (int i = 0; i < 12; ++i) { __builtin_amdgcn_sched_group_barrier(0x008, 1, 0); __builtin_amdgcn_sched_group_barrier(0x002, 2, 0); }
                }
            }
            SCHED();
        }
    }
#pragma unroll
    for (int c = 0; c < 8; ++c) CV_EPI(1, chw + 3, c);
    __builtin_amdgcn_s_setprio(0);
    WAIT_V(0); asm volatile("" :: "v"(touch));
    ST();
#undef CV_GX
#undef CV_GH
#undef CV_PUTX
#undef CV_SHIFT
#undef CV_PUTH
#undef CV_LOAD
#undef CV_EPI
}

constexpr int KB_STAGE = 32768, KC_N1 = 23, LDS_BYTES = 5 * KB_STAGE;
constexpr int SYNC_CNT = 5 * 32768 - 16;
struct G2Load { unsigned vA1, vA2, vB0, vB1; const char *cA, *cB; };
#define KD_B4(pb, pl) do { GLDS((pb) + g2.vB0, (pl) + 16384 + lw * 4096); GLDS((pb) + g2.vB0 + (size_t)4 * DM * 2, (pl) + 16384 + lw * 4096 + 1024); \
        GLDS((pb) + g2.vB1, (pl) + 16384 + lw * 4096 + 2048); GLDS((pb) + g2.vB1 + (size_t)4 * DM * 2, (pl) + 16384 + lw * 4096 + 3072); } while (0)
#define KD_ISSUE1A(s_, slot) do { const char* _a = g2.cA + (size_t)(KC_N1 * ks + (s_)) * 128 + g2.vA1; LAS unsigned char* _l = lds + (slot) * KB_STAGE; \
        GLDS_DEV(_a, _l + lw * 4096); GLDS_DEV(_a + (size_t)8 * FF * 2, _l + lw * 4096 + 1024); GLDS_DEV(_a + (size_t)16 * FF * 2, _l + lw * 4096 + 2048); GLDS_DEV(_a + (size_t)24 * FF * 2, _l + lw * 4096 + 3072); } while (0)
#define KD_ISSUE1B(s_, slot) do { const char* _b = g2.cB + (size_t)(KC_N1 * ks + (s_)) * 64 * DM * 2; KD_B4(_b, lds + (slot) * KB_STAGE); } while (0)
#define KD_ISSUE1(s_, slot) do { KD_ISSUE1A(s_, slot); KD_ISSUE1B(s_, slot); } while (0)
#define KD_ISSUE2(s_, slot) do { const int _kt = KC_N1 + (s_); const char* _a = g2.cA + (size_t)_kt * 128 + g2.vA2; const char* _b = g2.cB + (size_t)_kt * 64 * DM * 2; LAS unsigned char* _l = lds + (slot) * KB_STAGE; \
        GLDS_DEV(_a, _l + ks * 8192 + lw * 2048); GLDS_DEV(_a + (size_t)8 * FF * 2, _l + ks * 8192 + lw * 2048 + 1024); KD_B4(_b, _l); } while (0)
__device__ __forceinline__ G2Load gemm2_addr(const Params& P, int ks, int pm, int pn, int lw, int lane) {
    G2Load g2;
    const unsigned swA = (unsigned)(8 * ((lane & 7) ^ ((lane >> 3) & 7)) * 2);
    g2.vA1 = (unsigned)((lw * 32 + (lane >> 3)) * FF * 2) + swA;
    g2.vA2 = (unsigned)((ks * 64 + lw * 16 + (lane >> 3)) * FF * 2) + swA;
    g2.vB0 = (unsigned)(((lw * 16 + (lane >> 4)) * DM + 8 * ((lane & 15) ^ (((lane >> 4) & 3) << 2))) * 2);
    g2.vB1 = (unsigned)(((lw * 16 + 8 + (lane >> 4)) * DM + 8 * ((lane & 15) ^ ((((lane >> 4) & 3) << 2) | 2))) * 2);
    g2.cA = (const char*)P.hbuf + (size_t)pm * 128 * FF * 2;
    g2.cB = (const char*)P.wd + (size_t)pn * 128 * 2;
    return g2;
}

constexpr int HTB = 16384;
#define SA(b, h) (((b) * 2 + (h)) * HTB)
#define SB(b, h) ((4 + (b) * 2 + (h)) * HTB)
struct G1Addr { unsigned voffA, voffB; const char *cA, *cG, *cU; unsigned ldsw; };
#define STAGE_A(bufoff, h, kt) do { const char* _g = ga.cA + (h) * ((size_t)128 * DM * 2) + (size_t)(kt) * 128; \
        GLDS_DEV(_g + ga.voffA, lds + (bufoff) + ga.ldsw); GLDS_DEV(_g + (size_t)64 * DM * 2 + ga.voffA, lds + (bufoff) + ga.ldsw + 8192); } while (0)
#define STAGE_B(bufoff, base, kt) do { const char* _g = (base) + (size_t)(kt) * ((size_t)64 * FF * 2); \
        GLDS(_g + ga.voffB, lds + (bufoff) + ga.ldsw); GLDS(_g + (size_t)32 * FF * 2 + ga.voffB, lds + (bufoff) + ga.ldsw + 8192); } while (0)
__device__ __forceinline__ G1Addr gemm1_addr(const Params& P, int pm, int pn) {
    const int tid = threadIdx.x, wid = __builtin_amdgcn_readfirstlane(tid >> 6);
    G1Addr ga;
    ga.voffA = (unsigned)(((tid >> 3) * DM + 8 * ((tid & 7) ^ ((tid >> 3) & 7))) * 2);
    const int kb = tid >> 4;
    ga.voffB = (unsigned)((kb * FF + 8 * ((tid & 15) ^ (((kb & 3) << 2) | (((kb >> 3) & 1) << 1)))) * 2);
    ga.cA = (const char*)P.xn + (size_t)pm * 256 * DM * 2;
    ga.cG = (const char*)P.wg + (size_t)pn * 128 * 2;
    ga.cU = (const char*)P.wu + (size_t)pn * 128 * 2;
    ga.ldsw = (unsigned)wid * 1024u;
    return ga;
}
__device__ __forceinline__ void gemm1_block(const Params& P, LAS unsigned char* lds, const G1Addr& ga, const G2Load& g2, int ks, int pm, int pn, int& _sti) {
    const int tid = threadIdx.x, wid = __builtin_amdgcn_readfirstlane(tid >> 6), lane = tid & 63, wr = wid >> 2, wc = wid & 3, fr = lane & 15, fq = lane >> 4;
    constexpr int nt = DM / 64;
    const int q = (lane & 15) >> 2, p = lane & 3;
    const int aoff0 = (wr * 64 + fr) * 128 + 16 * ((fq) ^ (fr & 7)), aoff1 = (wr * 64 + fr) * 128 + 16 * ((4 + fq) ^ (fr & 7));
    const int boff0 = 256 * (8 * fq + q) + 16 * (4 * (wc ^ q) + 2 * (0 ^ (fq & 1)) + (p >> 1)) + 8 * (p & 1);
    const int boff1 = 256 * (8 * fq + q) + 16 * (4 * (wc ^ q) + 2 * (1 ^ (fq & 1)) + (p >> 1)) + 8 * (p & 1);
#define LDA(dst, b, h) do { _Pragma("unroll") for (int m = 0; m < 4; ++m) { \
        dst[m][0] = *(const LAS bf16x8*)(lds + SA(b, h) + aoff0 + m * 2048); dst[m][1] = *(const LAS bf16x8*)(lds + SA(b, h) + aoff1 + m * 2048); } } while (0)
#define LDB(dst, b, h) do { \
        TRRD(dst[0][0][0], bb0, SB(b, h) - SB(0, 0)); TRRD(dst[0][0][1], bb0, SB(b, h) - SB(0, 0) + 1024); TRRD(dst[1][0][0], bb1, SB(b, h) - SB(0, 0)); TRRD(dst[1][0][1], bb1, SB(b, h) - SB(0, 0) + 1024); \
        TRRD(dst[0][1][0], bb0, SB(b, h) - SB(0, 0) + 8192); TRRD(dst[0][1][1], bb0, SB(b, h) - SB(0, 0) + 9216); TRRD(dst[1][1][0], bb1, SB(b, h) - SB(0, 0) + 8192); TRRD(dst[1][1][1], bb1, SB(b, h) - SB(0, 0) + 9216); } while (0)
#define WAIT_LB(dst) asm volatile("s_waitcnt lgkmcnt(0)" : "+v"(dst[0][0][0]), "+v"(dst[0][0][1]), "+v"(dst[0][1][0]), "+v"(dst[0][1][1]), "+v"(dst[1][0][0]), "+v"(dst[1][0][1]), "+v"(dst[1][1][0]), "+v"(dst[1][1][1]) :: "memory")
#define MMA(ai, bj, At, Bt) do { __builtin_amdgcn_s_setprio(1); _Pragma("unroll") for (int m = 0; m < 4; ++m) _Pragma("unroll") for (int n = 0; n < 2; ++n) _Pragma("unroll") for (int k = 0; k < 2; ++k) \
        acc[ai][bj][m][n] = __builtin_amdgcn_mfma_f32_16x16x32_bf16(CAT(Bt[n][k][0], Bt[n][k][1]), At[m][k], acc[ai][bj][m][n], 0, 0, 0); __builtin_amdgcn_s_setprio(0); } while (0)
    f32x4 acc[2][2][4][2];
#pragma unroll
    for (int a = 0; a < 2; ++a)
#pragma unroll
        for (int b = 0; b < 2; ++b)
#pragma unroll
            for (int m = 0; m < 4; ++m)
#pragma unroll
                for (int n = 0; n < 2; ++n) acc[a][b][m][n] = (f32x4){0.f, 0.f, 0.f, 0.f};
    bf16x8 At[4][2]; bf16x4 B0[2][2][2], B1[2][2][2];
    const unsigned bb0 = (unsigned)(uintptr_t)(lds + SB(0, 0) + boff0), bb1 = (unsigned)(uintptr_t)(lds + SB(0, 0) + boff1);
    if (wr == 1) BAR();
    WAIT_V(2); WAIT_L(0); BAR(); BAR();
    ST();
    for (int t = 0; t < nt - 2; t += 2) {
        LDB(B0, 0, 0); SCHED(); LDA(At, 0, 0); STAGE_A(SA(1, 1), 1, t + 1);
        WAIT_L(8); BAR(); WAIT_LB(B0); SCHED(); MMA(0, 0, At, B0); BAR(); SCHED();
        LDB(B1, 0, 1); STAGE_B(SB(0, 0), ga.cG, t + 2);
        BAR(); WAIT_LB(B1); SCHED(); MMA(0, 1, At, B1); BAR();
        LDA(At, 0, 1); STAGE_A(SA(0, 0), 0, t + 2);
        BAR(); WAIT_L(0); MMA(1, 0, At, B0); BAR(); SCHED();
        STAGE_B(SB(0, 1), ga.cU, t + 2);
        WAIT_V(6); BAR(); MMA(1, 1, At, B1); BAR();
        LDB(B0, 1, 0); SCHED(); LDA(At, 1, 0); STAGE_A(SA(0, 1), 1, t + 2);
        WAIT_L(8); BAR(); WAIT_LB(B0); SCHED(); MMA(0, 0, At, B0); BAR(); SCHED();
        LDB(B1, 1, 1); STAGE_B(SB(1, 0), ga.cG, t + 3);
        BAR(); WAIT_LB(B1); SCHED(); MMA(0, 1, At, B1); BAR();
        LDA(At, 1, 1); STAGE_A(SA(1, 0), 0, t + 3);
        BAR(); WAIT_L(0); MMA(1, 0, At, B0); BAR(); SCHED();
        STAGE_B(SB(1, 1), ga.cU, t + 3);
        WAIT_V(6); BAR(); MMA(1, 1, At, B1); BAR();
    }
    {
        LDB(B0, 0, 0); SCHED(); LDA(At, 0, 0); STAGE_A(SA(1, 1), 1, nt - 1);
        WAIT_L(8); BAR(); WAIT_LB(B0); SCHED(); MMA(0, 0, At, B0); BAR(); SCHED();
        LDB(B1, 0, 1); BAR(); WAIT_LB(B1); SCHED(); MMA(0, 1, At, B1); BAR();
        LDA(At, 0, 1); BAR(); WAIT_L(0); MMA(1, 0, At, B0); BAR(); SCHED();
        WAIT_V(0); BAR(); MMA(1, 1, At, B1); BAR();
    }
    {
        LDB(B0, 1, 0); SCHED(); LDA(At, 1, 0); BAR(); WAIT_LB(B0); SCHED(); MMA(0, 0, At, B0); BAR(); SCHED();
        LDB(B1, 1, 1); BAR(); WAIT_LB(B1); SCHED(); MMA(0, 1, At, B1); BAR();
        LDA(At, 1, 1); BAR(); WAIT_L(0); MMA(1, 0, At, B0); MMA(1, 1, At, B1);
    }
    if (tid == 0) *(LAS unsigned*)(lds + SYNC_CNT) = 0u;
    if (wr == 0) BAR();
    WAIT_L(0); BAR();
    ST();
    if (wid >= 4) { const int lw = wid - 4; KD_ISSUE1B(0, 0); KD_ISSUE1B(1, 1); KD_ISSUE1B(2, 2); KD_ISSUE1B(3, 3); }
    int tz = threadIdx.x; asm volatile("" : "+v"(tz));
    const int el = tz & 63, efr = el & 15, efq = el >> 4;
#pragma unroll
    for (int ai = 0; ai < 2; ++ai)
#pragma unroll
        for (int m = 0; m < 4; ++m) {
            unsigned pk[2][2];
#pragma unroll
            for (int n = 0; n < 2; ++n) {
                float hv[4];
#pragma unroll
                for (int j = 0; j < 4; ++j) {
                    const float e = __builtin_amdgcn_exp2f(acc[ai][0][m][n][j] * -1.4426950408889634f);
                    hv[j] = acc[ai][1][m][n][j] * __builtin_amdgcn_rcpf(1.0f + e);
                }
                pk[n][0] = pack_bf16(hv[0], hv[1]); pk[n][1] = pack_bf16(hv[2], hv[3]);
            }
            const auto s0 = __builtin_amdgcn_permlane16_swap(pk[0][0], pk[1][0], false, false);
            const auto s1 = __builtin_amdgcn_permlane16_swap(pk[0][1], pk[1][1], false, false);
            const u32x4 w = {s0[0], s1[0], s0[1], s1[1]};
            const unsigned la = (unsigned)(uintptr_t)lds + (ai * 2 + wr) * KB_STAGE + (m * 16 + efr) * 256 + 16 * ((wc * 4 + 2 * (efq & 1) + (efq >> 1)) ^ efr);
            asm volatile("ds_write_b128 %0, %1" :: "v"(la), "v"(w) : "memory");
        }
    WAIT_L(0); BAR();
    const rsrc_t rh = mk_rsrc(P.hbuf, SEQ * FF * 2);
    {
        u32x4 w[8];
#pragma unroll
        for (int i = 0; i < 8; ++i) {
            const int row = 32 * wid + 4 * i + efq;
            RD128(w[i], (unsigned)(uintptr_t)lds + (row >> 6) * KB_STAGE + (row & 63) * 256 + 16 * (efr ^ (row & 15)), 0);
        }
        asm volatile("s_waitcnt lgkmcnt(0)" : "+v"(w[0]), "+v"(w[1]), "+v"(w[2]), "+v"(w[3]), "+v"(w[4]), "+v"(w[5]), "+v"(w[6]), "+v"(w[7]) :: "memory");
#pragma unroll
        for (int i = 0; i < 8; ++i) st16(rh, (unsigned)(((pm * 256 + 32 * wid + 4 * i + efq) * FF + pn * 128 + 8 * efr) * 2), w[i]);
    }
    ST();
#undef LDA
#undef LDB
#undef WAIT_LB
#undef MMA
}

#define KE_S01(XA, XB, YA, YB, ya0, ya1, yb0, yb1, yb2, yb3) do { \
        TRRD(YB[0][0][0], yb0, 0); TRRD(YB[0][0][1], yb0, 1024); RD128(YA[0][0], ya0, 0); TRRD(YB[0][1][0], yb1, 0); TRRD(YB[0][1][1], yb1, 1024); RD128(YA[0][1], ya0, 2048); TRRD(YB[0][2][0], yb2, 0); TRRD(YB[0][2][1], yb2, 1024); RD128(YA[0][2], ya0, 4096); TRRD(YB[0][3][0], yb3, 0); TRRD(YB[0][3][1], yb3, 1024); RD128(YA[0][3], ya0, 6144); TRRD(YB[1][0][0], yb0, 8192); TRRD(YB[1][0][1], yb0, 9216); RD128(YA[1][0], ya1, 0); TRRD(YB[1][1][0], yb1, 8192); TRRD(YB[1][1][1], yb1, 9216); RD128(YA[1][1], ya1, 2048); TRRD(YB[1][2][0], yb2, 8192); TRRD(YB[1][2][1], yb2, 9216); RD128(YA[1][2], ya1, 4096); TRRD(YB[1][3][0], yb3, 8192); TRRD(YB[1][3][1], yb3, 9216); RD128(YA[1][3], ya1, 6144); SCHED(); } while (0)
#define KE_S11(XA, XB, YA, YB, ya0, ya1, yb0, yb1, yb2, yb3) do { \
        KE_M(XA, XB, 0, 0, 0); SCHED(); TRRD(YB[0][0][0], yb0, 0); SCHED(); \
        KE_M(XA, XB, 0, 0, 1); SCHED(); TRRD(YB[0][0][1], yb0, 1024); SCHED(); \
        KE_M(XA, XB, 0, 0, 2); SCHED(); RD128(YA[0][0], ya0, 0); SCHED(); \
        KE_M(XA, XB, 0, 0, 3); SCHED(); TRRD(YB[0][1][0], yb1, 0); SCHED(); \
        KE_M(XA, XB, 0, 1, 0); SCHED(); TRRD(YB[0][1][1], yb1, 1024); SCHED(); \
        KE_M(XA, XB, 0, 1, 1); SCHED(); RD128(YA[0][1], ya0, 2048); SCHED(); \
        KE_M(XA, XB, 0, 1, 2); SCHED(); TRRD(YB[0][2][0], yb2, 0); SCHED(); \
        KE_M(XA, XB, 0, 1, 3); SCHED(); TRRD(YB[0][2][1], yb2, 1024); SCHED(); \
        KE_M(XA, XB, 0, 2, 0); SCHED(); RD128(YA[0][2], ya0, 4096); SCHED(); \
        KE_M(XA, XB, 0, 2, 1); SCHED(); TRRD(YB[0][3][0], yb3, 0); SCHED(); \
        KE_M(XA, XB, 0, 2, 2); SCHED(); TRRD(YB[0][3][1], yb3, 1024); SCHED(); \
        KE_M(XA, XB, 0, 2, 3); SCHED(); RD128(YA[0][3], ya0, 6144); SCHED(); \
        KE_M(XA, XB, 0, 3, 0); SCHED(); TRRD(YB[1][0][0], yb0, 8192); SCHED(); \
        KE_M(XA, XB, 0, 3, 1); SCHED(); TRRD(YB[1][0][1], yb0, 9216); SCHED(); \
        KE_M(XA, XB, 0, 3, 2); SCHED(); RD128(YA[1][0], ya1, 0); SCHED(); \
        KE_M(XA, XB, 0, 3, 3); SCHED(); TRRD(YB[1][1][0], yb1, 8192); SCHED(); \
        KE_M(XA, XB, 1, 0, 0); SCHED(); TRRD(YB[1][1][1], yb1, 9216); SCHED(); \
        KE_M(XA, XB, 1, 0, 1); SCHED(); RD128(YA[1][1], ya1, 2048); SCHED(); \
        KE_M(XA, XB, 1, 0, 2); SCHED(); TRRD(YB[1][2][0], yb2, 8192); SCHED(); \
        KE_M(XA, XB, 1, 0, 3); SCHED(); TRRD(YB[1][2][1], yb2, 9216); SCHED(); \
        KE_M(XA, XB, 1, 1, 0); SCHED(); RD128(YA[1][2], ya1, 4096); SCHED(); \
        KE_M(XA, XB, 1, 1, 1); SCHED(); TRRD(YB[1][3][0], yb3, 8192); SCHED(); \
        KE_M(XA, XB, 1, 1, 2); SCHED(); TRRD(YB[1][3][1], yb3, 9216); SCHED(); \
        KE_M(XA, XB, 1, 1, 3); SCHED(); RD128(YA[1][3], ya1, 6144); SCHED(); \
        KE_M(XA, XB, 1, 2, 0); SCHED(); SCHED(); \
        KE_M(XA, XB, 1, 2, 1); SCHED(); SCHED(); \
        KE_M(XA, XB, 1, 2, 2); SCHED(); SCHED(); \
        KE_M(XA, XB, 1, 2, 3); SCHED(); SCHED(); \
        KE_M(XA, XB, 1, 3, 0); SCHED(); SCHED(); \
        KE_M(XA, XB, 1, 3, 1); SCHED(); SCHED(); \
        KE_M(XA, XB, 1, 3, 2); SCHED(); SCHED(); \
        KE_M(XA, XB, 1, 3, 3); SCHED(); SCHED(); } while (0)
#define KE_S12(XA, XB, YA, YB, ya0, ya1, yb0, yb1, yb2, yb3) do { \
        KE_M(XA, XB, 0, 0, 0); SCHED(); TRRD(YB[0][0][0], yb0, 0); SCHED(); \
        KE_M(XA, XB, 0, 0, 1); SCHED(); TRRD(YB[0][0][1], yb0, 1024); SCHED(); \
        KE_M(XA, XB, 0, 0, 2); SCHED(); RD128(YA[0][0], ya0, 0); SCHED(); \
        KE_M(XA, XB, 0, 0, 3); SCHED(); TRRD(YB[0][1][0], yb1, 0); SCHED(); \
        KE_M(XA, XB, 0, 1, 0); SCHED(); TRRD(YB[0][1][1], yb1, 1024); SCHED(); \
        KE_M(XA, XB, 0, 1, 1); SCHED(); RD128(YA[0][1], ya0, 2048); SCHED(); \
        KE_M(XA, XB, 0, 1, 2); SCHED(); TRRD(YB[0][2][0], yb2, 0); SCHED(); \
        KE_M(XA, XB, 0, 1, 3); SCHED(); TRRD(YB[0][2][1], yb2, 1024); SCHED(); \
        KE_M(XA, XB, 0, 2, 0); SCHED(); TRRD(YB[0][3][0], yb3, 0); SCHED(); \
        KE_M(XA, XB, 0, 2, 1); SCHED(); TRRD(YB[0][3][1], yb3, 1024); SCHED(); \
        KE_M(XA, XB, 0, 2, 2); SCHED(); TRRD(YB[1][0][0], yb0, 8192); SCHED(); \
        KE_M(XA, XB, 0, 2, 3); SCHED(); TRRD(YB[1][0][1], yb0, 9216); SCHED(); \
        KE_M(XA, XB, 0, 3, 0); SCHED(); RD128(YA[1][0], ya1, 0); SCHED(); \
        KE_M(XA, XB, 0, 3, 1); SCHED(); TRRD(YB[1][1][0], yb1, 8192); SCHED(); \
        KE_M(XA, XB, 0, 3, 2); SCHED(); TRRD(YB[1][1][1], yb1, 9216); SCHED(); \
        KE_M(XA, XB, 0, 3, 3); SCHED(); RD128(YA[1][1], ya1, 2048); SCHED(); \
        KE_M(XA, XB, 1, 0, 0); SCHED(); TRRD(YB[1][2][0], yb2, 8192); SCHED(); \
        KE_M(XA, XB, 1, 0, 1); SCHED(); TRRD(YB[1][2][1], yb2, 9216); SCHED(); \
        KE_M(XA, XB, 1, 0, 2); SCHED(); TRRD(YB[1][3][0], yb3, 8192); SCHED(); \
        KE_M(XA, XB, 1, 0, 3); SCHED(); TRRD(YB[1][3][1], yb3, 9216); SCHED(); \
        KE_M(XA, XB, 1, 1, 0); SCHED(); SCHED(); \
        KE_M(XA, XB, 1, 1, 1); SCHED(); SCHED(); \
        KE_M(XA, XB, 1, 1, 2); SCHED(); SCHED(); \
        KE_M(XA, XB, 1, 1, 3); SCHED(); SCHED(); \
        KE_M(XA, XB, 1, 2, 0); SCHED(); SCHED(); \
        KE_M(XA, XB, 1, 2, 1); SCHED(); SCHED(); \
        KE_M(XA, XB, 1, 2, 2); SCHED(); SCHED(); \
        KE_M(XA, XB, 1, 2, 3); SCHED(); SCHED(); \
        KE_M(XA, XB, 1, 3, 0); SCHED(); SCHED(); \
        KE_M(XA, XB, 1, 3, 1); SCHED(); SCHED(); \
        KE_M(XA, XB, 1, 3, 2); SCHED(); SCHED(); \
        KE_M(XA, XB, 1, 3, 3); SCHED(); SCHED(); } while (0)
#define KE_S22(XA, XB, YA, YB, ya0, ya1, yb0, yb1, yb2, yb3) do { \
        KE_M(XA, XB, 0, 0, 0); SCHED(); TRRD(YB[0][0][0], yb0, 0); TRRD(YB[0][0][1], yb0, 1024); SCHED(); \
        KE_M(XA, XB, 0, 0, 1); SCHED(); RD128(YA[0][0], ya0, 0); TRRD(YB[0][1][0], yb1, 0); SCHED(); \
        KE_M(XA, XB, 0, 0, 2); SCHED(); TRRD(YB[0][1][1], yb1, 1024); RD128(YA[0][1], ya0, 2048); SCHED(); \
        KE_M(XA, XB, 0, 0, 3); SCHED(); TRRD(YB[0][2][0], yb2, 0); TRRD(YB[0][2][1], yb2, 1024); SCHED(); \
        KE_M(XA, XB, 0, 1, 0); SCHED(); TRRD(YB[0][3][0], yb3, 0); SCHED(); \
        KE_M(XA, XB, 0, 1, 1); SCHED(); TRRD(YB[0][3][1], yb3, 1024); SCHED(); \
        KE_M(XA, XB, 0, 1, 2); SCHED(); TRRD(YB[1][0][0], yb0, 8192); SCHED(); \
        KE_M(XA, XB, 0, 1, 3); SCHED(); TRRD(YB[1][0][1], yb0, 9216); SCHED(); \
        KE_M(XA, XB, 1, 0, 0); SCHED(); RD128(YA[1][0], ya1, 0); SCHED(); \
        KE_M(XA, XB, 1, 0, 1); SCHED(); TRRD(YB[1][1][0], yb1, 8192); SCHED(); \
        KE_M(XA, XB, 1, 0, 2); SCHED(); TRRD(YB[1][1][1], yb1, 9216); SCHED(); \
        KE_M(XA, XB, 1, 0, 3); SCHED(); RD128(YA[1][1], ya1, 2048); SCHED(); \
        KE_M(XA, XB, 1, 1, 0); SCHED(); TRRD(YB[1][2][0], yb2, 8192); SCHED(); \
        KE_M(XA, XB, 1, 1, 1); SCHED(); TRRD(YB[1][2][1], yb2, 9216); SCHED(); \
        KE_M(XA, XB, 1, 1, 2); SCHED(); TRRD(YB[1][3][0], yb3, 8192); SCHED(); \
        KE_M(XA, XB, 1, 1, 3); SCHED(); TRRD(YB[1][3][1], yb3, 9216); SCHED(); } while (0)
#define KE_S20(XA, XB, YA, YB, ya0, ya1, yb0, yb1, yb2, yb3) do { \
        KE_M(XA, XB, 0, 0, 0); SCHED(); SCHED(); \
        KE_M(XA, XB, 0, 0, 1); SCHED(); SCHED(); \
        KE_M(XA, XB, 0, 0, 2); SCHED(); SCHED(); \
        KE_M(XA, XB, 0, 0, 3); SCHED(); SCHED(); \
        KE_M(XA, XB, 0, 1, 0); SCHED(); SCHED(); \
        KE_M(XA, XB, 0, 1, 1); SCHED(); SCHED(); \
        KE_M(XA, XB, 0, 1, 2); SCHED(); SCHED(); \
        KE_M(XA, XB, 0, 1, 3); SCHED(); SCHED(); \
        KE_M(XA, XB, 1, 0, 0); SCHED(); SCHED(); \
        KE_M(XA, XB, 1, 0, 1); SCHED(); SCHED(); \
        KE_M(XA, XB, 1, 0, 2); SCHED(); SCHED(); \
        KE_M(XA, XB, 1, 0, 3); SCHED(); SCHED(); \
        KE_M(XA, XB, 1, 1, 0); SCHED(); SCHED(); \
        KE_M(XA, XB, 1, 1, 1); SCHED(); SCHED(); \
        KE_M(XA, XB, 1, 1, 2); SCHED(); SCHED(); \
        KE_M(XA, XB, 1, 1, 3); SCHED(); SCHED(); } while (0)

struct G2Epi { u32x4 ex[4]; unsigned es; };
__device__ __forceinline__ void gemm2_epi_request(const Params& P, G2Epi& ep, int ks, int pm, int pn, int lw, int lane) {
#pragma unroll
    for (int i = 0; i < 4; ++i) {
        const int xr = (lw * 4 + i) * 4 + (lane >> 4), xc = (lane & 15) ^ (xr & 15);
        const char* q = (const char*)P.x + ((size_t)(pm * 128 + ks * 64 + xr) * DM + pn * 128 + 8 * xc) * 2;
        asm volatile("global_load_dwordx4 %0, %1, off" : "=&v"(ep.ex[i]) : "v"(q) : "memory");
    }
    const char* q = (const char*)P.stats + (size_t)pm * 128 * 8 + (lw * 64 + lane) * 4;
    asm volatile("global_load_dword %0, %1, off sc1" : "=&v"(ep.es) : "v"(q) : "memory");
}
__device__ __forceinline__ void gemm2_loader(const Params& P, LAS unsigned char* lds, const G2Load& g2, G2Epi& ep, int ks, int pm, int pn, int lw, int lane) {
    const int spare = (1 - ks) * 8192;
    u32x4 ey[4]; u32x4 (&ex)[4] = ep.ex; unsigned& es = ep.es;
#define KD_LDY(i) do { const int row = (lw * 4 + (i)) * 8 + (lane >> 3), ck = (lane & 7) ^ (((row >> 1) & 3) << 1); \
        const char* _p = (const char*)P.y2T + ((size_t)(pn * 128 + row) * SEQ + pm * 128 + ks * 64 + 8 * ck) * 2; \
        asm volatile("global_load_dwordx4 %0, %1, off sc1" : "=&v"(ey[i]) : "v"(_p) : "memory"); } while (0)
#define KD_PUT() do { asm volatile("" : "+v"(ey[0]), "+v"(ey[1]), "+v"(ey[2]), "+v"(ey[3]), "+v"(ex[0]), "+v"(ex[1]), "+v"(ex[2]), "+v"(ex[3]), "+v"(es)); \
        _Pragma("unroll") for (int i = 0; i < 4; ++i) { \
            *(LAS u32x4*)(lds + (2 + (lw >> 1)) * KB_STAGE + spare + ((lw & 1) * 4 + i) * 1024 + lane * 16) = ey[i]; \
            *(LAS u32x4*)(lds + (lw >> 1) * KB_STAGE + spare + ((lw & 1) * 4 + i) * 1024 + lane * 16) = ex[i]; } \
        *(LAS unsigned*)(lds + 4 * KB_STAGE + spare + 1024 + (lw * 64 + lane) * 4) = es; } while (0)
        KD_ISSUE1A(0, 0); KD_ISSUE1A(1, 1); KD_ISSUE1A(2, 2); KD_ISSUE1A(3, 3); KD_LDY(0); KD_LDY(1); KD_LDY(2); KD_LDY(3);
        WAIT_V(16); BAR();
        WAIT_V(12); BAR(); KD_ISSUE1(4, 4);
        WAIT_V(16); BAR(); KD_ISSUE1(5, 0);
        WAIT_V(20); BAR(); KD_ISSUE1(6, 1);
        WAIT_V(16); BAR(); KD_ISSUE1(7, 2);
        WAIT_V(16); BAR(); KD_ISSUE1(8, 3);
        WAIT_V(16); BAR(); KD_ISSUE1(9, 4);
        WAIT_V(16); BAR(); KD_ISSUE1(10, 0);
        WAIT_V(16); BAR(); KD_ISSUE1(11, 1);
        WAIT_V(16); BAR(); KD_ISSUE1(12, 2);
        WAIT_V(16); BAR(); KD_ISSUE1(13, 3);
        WAIT_V(16); BAR(); KD_ISSUE1(14, 4);
        WAIT_V(16); BAR(); KD_ISSUE1(15, 0);
        WAIT_V(16); BAR(); KD_ISSUE1(16, 1);
        WAIT_V(16); BAR(); KD_ISSUE1(17, 2);
        WAIT_V(16); BAR(); KD_ISSUE1(18, 3);
        WAIT_V(16); BAR(); KD_ISSUE1(19, 4);
        WAIT_V(16); BAR(); KD_ISSUE1(20, 0);
        WAIT_V(16); BAR(); KD_ISSUE1(21, 1);
        WAIT_V(16); BAR(); KD_ISSUE1(22, 2);
        WAIT_V(16); BAR(); KD_ISSUE2(23, 3);
        WAIT_V(14); BAR(); KD_ISSUE2(24, 4);
        WAIT_V(12); BAR(); KD_ISSUE2(25, 0);
        WAIT_V(12); BAR(); KD_ISSUE2(26, 1); BAR();
        WAIT_V(12); BAR(); KD_PUT(); KD_ISSUE2(27, 2); WAIT_L(0);
        WAIT_V(12); BAR(); KD_ISSUE2(28, 3);
        WAIT_V(12); BAR(); KD_ISSUE2(29, 4);
        WAIT_V(12); BAR(); KD_ISSUE2(30, 0);
        WAIT_V(12); BAR(); KD_ISSUE2(31, 1);
        WAIT_V(12); BAR(); KD_ISSUE2(32, 2);
        WAIT_V(12); BAR();
        WAIT_V(6); BAR();
        WAIT_V(0); BAR();
#undef KD_LDY
#undef KD_PUT
}
__device__ __forceinline__ void gemm2_mult(const Params& P, LAS unsigned char* lds, int ks, int pm, int pn, int wid, int lane, u64 tag, int& _sti) {
    const int tid = threadIdx.x, spare = (1 - ks) * 8192;
    const int wr = wid >> 1, wc = wid & 1, rg = (wr != ks) ? 1 : 0, fr = lane & 15, fq = lane >> 4, q = (lane & 15) >> 2, p = lane & 3;
    const unsigned ldsb = (unsigned)(uintptr_t)lds;
    const int a1row = wr * 64 + fr, a2row = ks * 64 + rg * 32 + fr;
    const unsigned a1k0 = ldsb + a1row * 128 + 16 * ((0 + fq) ^ (fr & 7)), a1k1 = ldsb + a1row * 128 + 16 * ((4 + fq) ^ (fr & 7));
    const unsigned a2k0 = ldsb + a2row * 128 + 16 * ((0 + fq) ^ (fr & 7)), a2k1 = ldsb + a2row * 128 + 16 * ((4 + fq) ^ (fr & 7));
    unsigned bo[4];
#pragma unroll
    for (int n = 0; n < 4; ++n) bo[n] = ldsb + 16384 + 256 * (8 * fq + q) + 16 * ((8 * wc + 2 * n + (p >> 1)) ^ (4 * q + 2 * (fq & 1))) + 8 * (p & 1);
#define KE_M(Af, Bf, kk, m, n) acc[m][n] = __builtin_amdgcn_mfma_f32_16x16x32_bf16(CAT(Bf[kk][n][0], Bf[kk][n][1]), Af[kk][m], acc[m][n], 0, 0, 0)
#define KE_WAITL(Af, Bf, NM) do { asm volatile("s_waitcnt lgkmcnt(0)" : "+v"(Bf[0][0][0]), "+v"(Bf[0][0][1]), "+v"(Bf[0][1][0]), "+v"(Bf[0][1][1]), "+v"(Bf[0][2][0]), "+v"(Bf[0][2][1]), "+v"(Bf[0][3][0]), "+v"(Bf[0][3][1]), \
        "+v"(Bf[1][0][0]), "+v"(Bf[1][0][1]), "+v"(Bf[1][1][0]), "+v"(Bf[1][1][1]), "+v"(Bf[1][2][0]), "+v"(Bf[1][2][1]), "+v"(Bf[1][3][0]), "+v"(Bf[1][3][1]) :: "memory"); \
        asm volatile("" : "+v"(Af[0][0]), "+v"(Af[0][1]), "+v"(Af[1][0]), "+v"(Af[1][1])); if ((NM) == 4) asm volatile("" : "+v"(Af[0][2]), "+v"(Af[0][3]), "+v"(Af[1][2]), "+v"(Af[1][3])); } while (0)
    f32x4 acc[4][4];
#pragma unroll
    for (int m = 0; m < 4; ++m)
#pragma unroll
        for (int n = 0; n < 4; ++n) acc[m][n] = (f32x4){0.f, 0.f, 0.f, 0.f};
    bf16x8 PA[2][4], QA[2][4]; bf16x4 PB[2][4][2], QB[2][4][2];
    int so = 0;
#define KD_ADDR(AK0, AK1) const unsigned _a0 = (AK0) + so, _a1 = (AK1) + so, _b0 = bo[0] + so, _b1 = bo[1] + so, _b2 = bo[2] + so, _b3 = bo[3] + so
#define KD_NEXT() so = (so == 4 * KB_STAGE) ? 0 : so + KB_STAGE
    BAR();
    ST();
    { KD_ADDR(a1k0, a1k1); KE_S01(QA, QB, PA, PB, _a0, _a1, _b0, _b1, _b2, _b3); KD_NEXT(); }
#define KE_STEP(MAC, NM, AK0, AK1, XA, XB, YA, YB) do { KD_ADDR(AK0, AK1); unsigned _u0 = _a0, _u1 = _a1, _v0 = _b0, _v1 = _b1, _v2 = _b2, _v3 = _b3; \
        asm volatile("" : "+v"(_u0), "+v"(_u1), "+v"(_v0), "+v"(_v1), "+v"(_v2), "+v"(_v3)); \
        KE_WAITL(XA, XB, NM); BAR(); SCHED(); MAC(XA, XB, YA, YB, _u0, _u1, _v0, _v1, _v2, _v3); KD_NEXT(); } while (0)
    const int erow = pm * 128 + ks * 64 + rg * 32 + fr;
    const int ecol = pn * 128 + wc * 64 + 16 * (fq & 1) + 8 * (fq >> 1);
    for (int s = 0; s < 22; s += 2) {
        KE_STEP(KE_S11, 4, a1k0, a1k1, PA, PB, QA, QB);
        KE_STEP(KE_S11, 4, a1k0, a1k1, QA, QB, PA, PB);
    }
    ST();
    KE_STEP(KE_S12, 4, a2k0, a2k1, PA, PB, QA, QB);
    const rsrc_t rp = mk_rsrc(P.part, 256 * 16384);
    LAS unsigned* cnt = (LAS unsigned*)(lds + 4 * KB_STAGE + spare);
    {
        LAS f32x4* ho = (LAS f32x4*)(lds + wc * KB_STAGE + spare);
        if (rg == 0) {
#pragma unroll
            for (int m = 0; m < 2; ++m)
#pragma unroll
                for (int n = 0; n < 4; ++n) ho[(m * 4 + n) * 64 + lane] = acc[2 + m][n];
            if (tid == (ks << 7)) *cnt = 0u;
        } else {
#pragma unroll
            for (int m = 0; m < 4; ++m)
#pragma unroll
                for (int h = 0; h < 2; ++h) {
                    const f32x4 a0 = acc[m][2 * h], a1 = acc[m][2 * h + 1];
                    st16(rp, blockIdx.x * 16384u + (unsigned)(wc * 8192 + (m * 2 + h) * 1024 + lane * 16),
                         (u32x4){pack_bf16(a0[0], a0[1]), pack_bf16(a0[2], a0[3]), pack_bf16(a1[0], a1[1]), pack_bf16(a1[2], a1[3])});
                }
        }
        WAIT_L(0); BAR();
        if (rg == 1) {
#pragma unroll
            for (int m = 0; m < 2; ++m)
#pragma unroll
                for (int n = 0; n < 4; ++n) acc[m][n] = ho[(m * 4 + n) * 64 + lane];
        }
    }
    KE_STEP(KE_S22, 2, a2k0, a2k1, QA, QB, PA, PB);
    KE_STEP(KE_S22, 2, a2k0, a2k1, PA, PB, QA, QB);
    KE_STEP(KE_S22, 2, a2k0, a2k1, QA, QB, PA, PB);
    KE_STEP(KE_S22, 2, a2k0, a2k1, PA, PB, QA, QB);
    if (rg == 1) {
        WAIT_V(0);
        if (lane == 0) {
            const unsigned n = __hip_atomic_fetch_add(cnt, 1u, __ATOMIC_RELAXED, __HIP_MEMORY_SCOPE_WORKGROUP);
            if (n == 1u) signal(P.sync + SY_P + blockIdx.x * 16, tag);
        }
    }
    KE_STEP(KE_S22, 2, a2k0, a2k1, QA, QB, PA, PB);
    KE_STEP(KE_S22, 2, a2k0, a2k1, PA, PB, QA, QB);
    KE_STEP(KE_S22, 2, a2k0, a2k1, QA, QB, PA, PB);
    u64 pf;
    {
        const u64* pfl = P.sync + SY_P + (blockIdx.x ^ 1) * 16;
        asm volatile("global_load_dwordx2 %0, %1, off sc1" : "=&v"(pf) : "v"(pfl) : "memory");
    }
    KE_STEP(KE_S22, 2, a2k0, a2k1, PA, PB, QA, QB);
    KE_STEP(KE_S22, 2, a2k0, a2k1, QA, QB, PA, PB);
    ST();
    asm volatile("s_waitcnt vmcnt(0)" : "+v"(pf) :: "memory");
    if (!__all(pf == tag)) poll_words(P.sync + SY_P + (blockIdx.x ^ 1) * 16, true, tag);
    asm volatile("" ::: "memory");
    u32x4 pin[2][2];
#pragma unroll
    for (int m = 0; m < 2; ++m)
#pragma unroll
        for (int h = 0; h < 2; ++h) {
            const unsigned o = (blockIdx.x ^ 1) * 16384u + (unsigned)(wc * 8192 + ((2 * rg + m) * 2 + h) * 1024 + lane * 16);
            asm volatile("buffer_load_dwordx4 %0, %1, %2, 0 offen sc1" : "=&v"(pin[m][h]) : "v"(o), "s"(rp) : "memory");
        }
    KE_WAITL(PA, PB, 2); SCHED();
    KE_S20(PA, PB, QA, QB, 0, 0, 0, 0, 0, 0);
    ST();
    bf16x4 yv[2][2][2]; u32x4 xv[2][2]; f32x2 st[2];
#pragma unroll
    for (int m = 0; m < 2; ++m) {
        st[m] = *(const LAS f32x2*)(lds + 4 * KB_STAGE + spare + 1024 + (ks * 64 + rg * 32 + 16 * m + fr) * 8);
#pragma unroll
        for (int h = 0; h < 2; ++h) {
            const int xr = 16 * m + fr, xc = 8 * wc + 4 * h + 2 * (fq & 1) + (fq >> 1);
            xv[m][h] = *(const LAS u32x4*)(lds + rg * KB_STAGE + spare + xr * 256 + 16 * (xc ^ (xr & 15)));
        }
    }
#pragma unroll
    for (int m = 0; m < 2; ++m)
#pragma unroll
        for (int h = 0; h < 2; ++h) {
            const int crow = 32 * h + 16 * (fq & 1) + 8 * (fq >> 1) + q;
            const int tck = (rg * 32 + 16 * m) / 8 + (p >> 1);
            const unsigned yb = ldsb + (2 + wc) * KB_STAGE + spare + crow * 128 + 8 * (p & 1);
            TRRD(yv[m][h][0], yb + 16 * (tck ^ (((crow >> 1) & 3) << 1)), 0);
            TRRD(yv[m][h][1], yb + 16 * (tck ^ ((((crow + 4) >> 1) & 3) << 1)), 512);
        }
    asm volatile("s_waitcnt vmcnt(0) lgkmcnt(0)" : "+v"(pin[0][0]), "+v"(pin[0][1]), "+v"(pin[1][0]), "+v"(pin[1][1]), "+v"(yv[0][0][0]), "+v"(yv[0][0][1]), "+v"(yv[0][1][0]), "+v"(yv[0][1][1]),
                 "+v"(yv[1][0][0]), "+v"(yv[1][0][1]), "+v"(yv[1][1][0]), "+v"(yv[1][1][1]) :: "memory");
    const rsrc_t ro = mk_rsrc(P.out, SEQ * DM * 2);
#pragma unroll
    for (int m = 0; m < 2; ++m) {
        const float rs = st[m][1], mrs = -st[m][0] * rs;
        u32x4 res[2];
#pragma unroll
        for (int h = 0; h < 2; ++h) {
            const u32x4 w = pin[m][h];
            const f32x4 a0 = acc[m][2 * h] + (f32x4){bf_lo(w[0]), bf_hi(w[0]), bf_lo(w[1]), bf_hi(w[1])};
            const f32x4 a1 = acc[m][2 * h + 1] + (f32x4){bf_lo(w[2]), bf_hi(w[2]), bf_lo(w[3]), bf_hi(w[3])};
            float o[8];
#pragma unroll
            for (int j = 0; j < 4; ++j) {
                const auto sw = __builtin_amdgcn_permlane16_swap(__float_as_uint(a0[j]), __float_as_uint(a1[j]), false, false);
                o[j] = __uint_as_float(sw[0]); o[4 + j] = __uint_as_float(sw[1]);
            }
#pragma unroll
            for (int j = 0; j < 8; ++j) {
                const unsigned xw = xv[m][h][j >> 1];
                const float xf = (j & 1) ? bf_hi(xw) : bf_lo(xw);
                const float yf = __uint_as_float((unsigned)(u16)yv[m][h][j >> 2][j & 3] << 16);
                o[j] += xf + (xf * rs + mrs) * yf;
            }
            res[h] = (u32x4){pack_bf16(o[0], o[1]), pack_bf16(o[2], o[3]), pack_bf16(o[4], o[5]), pack_bf16(o[6], o[7])};
        }
        u32x4 sa, sb;
#pragma unroll
        for (int i = 0; i < 4; ++i) {
            const unsigned y = (unsigned)__builtin_amdgcn_update_dpp(0, (int)res[1][i], 0x128  , 0xf, 0xf, false);
            sa[i] = fr < 8 ? res[0][i] : y; sb[i] = fr < 8 ? y : res[0][i];
        }
        const int rbase = erow - fr + 16 * m + (fr & 7), cbase = ecol;
        st16(ro, (unsigned)((rbase * DM + cbase + (fr < 8 ? 0 : 32)) * 2), sa);
        st16(ro, (unsigned)(((rbase + 8) * DM + cbase + (fr < 8 ? 32 : 0)) * 2), sb);
    }
    ST();
#undef KE_M
#undef KE_WAITL
#undef KE_STEP
#undef KD_ADDR
#undef KD_NEXT
}

constexpr int PA_K = 0, PA_V = 17408, PA_T = 34816, PA_C = 51200;
__global__ __launch_bounds__(512, 2) void fused_kernel(Params P) {
    extern __shared__ __attribute__((aligned(16))) unsigned char shm[];
    LAS unsigned char* lds = (LAS unsigned char*)shm;
    int _sti = 0;
    ST();
    const int tid = threadIdx.x, wid = __builtin_amdgcn_readfirstlane(tid >> 6), lane = tid & 63;
    const int b = blockIdx.x, xcd = b & 7, bi = b >> 3;
    const u64 tag = __hip_atomic_load((gu64*)(P.sync + SY_EPOCH), __ATOMIC_RELAXED, __HIP_MEMORY_SCOPE_AGENT) + 1ull;
    const bool is_gemm = bi < 28;
    const int pm1 = 4 * (xcd & 1) + (bi & 3), pn1 = 7 * (xcd >> 1) + (bi >> 2), cb = xcd * 4 + (bi - 28);
    const G1Addr ga = gemm1_addr(P, pm1, is_gemm ? pn1 : 0);
    ConvPre cpre;
    {
        const int row = b * 8 + wid, cg = b & 15, rt = b >> 4;
        const u16* xr = P.x + (size_t)row * DM;
        u32x4 a, bq; u32x2 wt;
        asm volatile("global_load_dwordx4 %0, %1, off" X_POL : "=&v"(a) : "v"(xr + lane * 8) : "memory");
        asm volatile("global_load_dwordx4 %0, %1, off offset:1024" X_POL : "=&v"(bq) : "v"(xr + lane * 8) : "memory");
        asm volatile("global_load_dwordx2 %0, %1, off" : "=&v"(wt) : "v"(P.shk + (size_t)(cg * 64 + lane) * 4) : "memory");
#pragma unroll
        for (int i = 0; i < 2; ++i) {
            const int j = wid + 8 * i, kr = 128 * rt - 8 + 8 * j + (lane >> 3);
            __builtin_amdgcn_global_load_lds((const GAS unsigned*)(P.kin + (size_t)(kr < 0 ? 0 : kr) * DM + cg * 64 + (lane & 7) * 8), (LAS unsigned*)(lds + PA_K + j * 1024), 16, 0, 2);
            __builtin_amdgcn_global_load_lds((const GAS unsigned*)(P.vin + (size_t)(128 * rt + 8 * j + (lane >> 3)) * DM + cg * 64 + (lane & 7) * 8), (LAS unsigned*)(lds + PA_V + j * 1024), 16, 0, 2);
        }
        if (wid == 0) __builtin_amdgcn_global_load_lds((const GAS unsigned*)(P.kin + (size_t)(128 * rt + 120 + (lane >> 3)) * DM + cg * 64 + (lane & 7) * 8), (LAS unsigned*)(lds + PA_K + 16 * 1024), 16, 0, 2);
        WAIT_V(4);
        asm volatile("" : "+v"(a), "+v"(bq), "+v"(wt));
        float v[16];
#pragma unroll
        for (int i = 0; i < 4; ++i) { v[2 * i] = bf_lo(a[i]); v[2 * i + 1] = bf_hi(a[i]); v[8 + 2 * i] = bf_lo(bq[i]); v[9 + 2 * i] = bf_hi(bq[i]); }
        float s = 0.f;
#pragma unroll
        for (int i = 0; i < 16; ++i) s += v[i];
        const float mean = wave_sum(s) * (1.0f / DM);
        float q = 0.f;
#pragma unroll
        for (int i = 0; i < 16; ++i) { const float d = v[i] - mean; q += d * d; }
        const float mb = bf_round(mean), vb = bf_round(wave_sum(q) * (1.0f / DM));
        const float sb = bf_round(sqrtf(bf_round(vb + 1e-5f)));
        const float rs = 1.0f / sb;
        u32x4 oa, ob;
#pragma unroll
        for (int i = 0; i < 4; ++i) {
            oa[i] = pack_bf16(bf_round(v[2 * i] - mb) * rs, bf_round(v[2 * i + 1] - mb) * rs);
            ob[i] = pack_bf16(bf_round(v[8 + 2 * i] - mb) * rs, bf_round(v[9 + 2 * i] - mb) * rs);
        }
        const float w0 = bf_lo(wt[0]), w1 = bf_hi(wt[0]), w2 = bf_lo(wt[1]), w3 = bf_hi(wt[1]);
        ST();
        const rsrc_t rx = mk_rsrc(P.xn, SEQ * DM * 2);
        st16(rx, (unsigned)(row * DM * 2 + lane * 16), oa); st16(rx, (unsigned)(row * DM * 2 + 1024 + lane * 16), ob);
        st8(mk_rsrc(P.stats, SEQ * 8), lane == 0 ? (unsigned)(row * 8) : 0xfffffff0u, (u32x2){__float_as_uint(mb), __float_as_uint(rs)});
        if (!is_gemm) { conv_prefetch(P, cb, cpre); WAIT_V(4); } else { WAIT_V(0); }
        WAIT_L(0); BAR();
        if (tid == 0) signal(P.sync + SY_X + b, tag);
        if (is_gemm) { STAGE_B(SB(0, 0), ga.cG, 0); STAGE_B(SB(0, 1), ga.cU, 0); STAGE_B(SB(1, 0), ga.cG, 1); STAGE_B(SB(1, 1), ga.cU, 1); }
        if (tid == 0) *(LAS unsigned*)(lds + PA_C + 4) = 0u;
        ST();
        float kf[19], kv[16];
#pragma unroll
        for (int i = 0; i < 19; ++i) kf[i] = bf_lo((unsigned)*(const LAS u16*)(lds + PA_K + (16 * wid + 5 + i) * 128 + lane * 2));
        if (rt == 0 && wid == 0) { kf[0] = 0.f; kf[1] = 0.f; kf[2] = 0.f; }
#pragma unroll
        for (int i = 0; i < 16; ++i) {
            const float vf = bf_lo((unsigned)*(const LAS u16*)(lds + PA_V + (16 * wid + i) * 128 + lane * 2));
            kv[i] = (kf[i] * w3 + kf[i + 1] * w2 + kf[i + 2] * w1 + kf[i + 3] * w0) * vf;
        }
#pragma unroll
        for (int j = 0; j < 2; ++j) {
            u32x4 o;
#pragma unroll
            for (int i = 0; i < 4; ++i) o[i] = pack_bf16(kv[8 * j + 7 - 2 * i], kv[8 * j + 6 - 2 * i]);
            *(LAS u32x4*)(lds + PA_T + lane * 256 + 16 * ((2 * wid + j) ^ (lane & 15))) = o;
        }
        ST();
        WAIT_L(0); BAR();
        u32x4 kt[2];
#pragma unroll
        for (int i = 0; i < 2; ++i) { const int ch = 8 * wid + 4 * i + (lane >> 4), ck = lane & 15; kt[i] = *(const LAS u32x4*)(lds + PA_T + ch * 256 + 16 * (ck ^ (ch & 15))); }
        const rsrc_t rk = mk_rsrc(P.kvT, SEQ * DM * 2);
#define PA_PUTK() do { _Pragma("unroll") for (int i = 0; i < 2; ++i) st16(rk, (unsigned)(((cg * 64 + 8 * wid + 4 * i + (lane >> 4)) * SEQ + 128 * rt + 8 * (lane & 15)) * 2), kt[i]); } while (0)
        if (is_gemm) {
            if (wid != 0) PA_PUTK();
            if (wid == 0) { poll_words(P.sync + SY_X + pm1 * 32 + (lane & 31), true, tag); PA_PUTK(); }
            WAIT_L(0); BAR();
            STAGE_A(SA(0, 0), 0, 0); STAGE_A(SA(0, 1), 1, 0); STAGE_A(SA(1, 0), 0, 1);
            WAIT_V(6);
        } else {
            PA_PUTK();
            WAIT_V(0);
        }
        if (last_wave((LAS unsigned*)(lds + PA_C + 4), 8) && lane == 0) signal(P.sync + SY_K + (b & 15) * 16 + (b >> 4), tag);
#undef PA_PUTK
        ST();
    }
    ST();
    const int ks = xcd & 1, pm2 = 8 * ((xcd >> 1) & 1) + (bi & 7), pn2 = 4 * (xcd >> 2) + (bi >> 3);
    const G2Load g2 = gemm2_addr(P, ks, pm2, pn2, wid & 3, lane);
    if (is_gemm) {
        ST();
        gemm1_block(P, lds, ga, g2, ks, pm1, pn1, _sti);
    } else {
        if (wid == 0) poll_words(P.sync + SY_K + (cb >> 1) * 16 + (lane & 15), true, tag);
        BAR();
        ST();
        ST();
        asm volatile("" : "+v"(cpre.nh[0]), "+v"(cpre.nh[1]), "+v"(cpre.nh[2]), "+v"(cpre.nh[3]));
        conv_block(P, lds, cb, cpre, _sti);
        WAIT_L(0); BAR();
        if (tid == 0) *(LAS unsigned*)(lds + SYNC_CNT) = 0u;
        WAIT_L(0); BAR();
        if (wid >= 4) { const int lw = wid - 4; KD_ISSUE1B(0, 0); KD_ISSUE1B(1, 1); KD_ISSUE1B(2, 2); KD_ISSUE1B(3, 3); }
        ST();
    }
    if (wid >= 4 && !is_gemm) { WAIT_V(16); } else { WAIT_V(0); }
    if (last_wave((LAS unsigned*)(lds + SYNC_CNT), 8) && lane == 0) signal(is_gemm ? P.sync + SY_H + pm1 * 32 + pn1 : P.sync + SY_Y + cb, tag);
    G2Epi ep;
    if (wid > 4) { poll_words(P.sync + SY_X + (pm2 >> 1) * 32 + (lane & 31), true, tag); gemm2_epi_request(P, ep, ks, pm2, pn2, wid - 4, lane); }
    if (wid == 4) {
        poll_words(lane < 28 ? P.sync + SY_H + (pm2 >> 1) * 32 + lane : P.sync + SY_Y + 4 * pn2 + (lane & 3), lane < 32, tag);
        gemm2_epi_request(P, ep, ks, pm2, pn2, 0, lane);
    }
    WAIT_L(0); BAR();
    ST();
    if (wid >= 4) {
        gemm2_loader(P, lds, g2, ep, ks, pm2, pn2, wid - 4, lane);
        if (b == 0 && wid == 4) {
#pragma unroll
            for (int i = 0; i < 4; ++i) poll_words(P.sync + SY_X + i * 64 + lane, true, tag);
            if (lane == 0) signal(P.sync + SY_EPOCH, tag);
        }
    } else {
        gemm2_mult(P, lds, ks, pm2, pn2, wid, lane, tag, _sti);
    }
}

extern "C" void kernel_launch(void* const* d_in, const int* in_sizes, int n_in, void* d_out, int out_size, void* d_ws, size_t ws_size, hipStream_t stream) {
    unsigned char* ws = (unsigned char*)d_ws;
    const size_t MB = 1u << 20;
    Params P;
    P.x = (const u16*)d_in[0]; P.kin = (const u16*)d_in[1]; P.vin = (const u16*)d_in[2]; P.shk = (const u16*)d_in[3]; P.ssmk = (const u16*)d_in[4]; P.ssmD = (const u16*)d_in[5];
    P.wg = (const u16*)d_in[7]; P.wu = (const u16*)d_in[8]; P.wd = (const u16*)d_in[9];
    P.out = (u16*)d_out;
    P.xn = (u16*)(ws + 0 * MB); P.kvT = (u16*)(ws + 4 * MB); P.y2T = (u16*)(ws + 8 * MB); P.hbuf = (u16*)(ws + 16 * MB);
    P.stats = (f32x2*)(ws + 12 * MB); P.part = (float*)(ws + 32 * MB); P.sync = (u64*)(ws + 13 * MB); P.stamps = (u64*)(ws + 64 * MB);
    (void)hipFuncSetAttribute((const void*)fused_kernel, hipFuncAttributeMaxDynamicSharedMemorySize, LDS_BYTES);
    fused_kernel<<<256, 512, LDS_BYTES, stream>>>(P);
}
```

```cpp
#include <hip/hip_runtime.h>
#include <hip/hip_bf16.h>
#include <stdint.h>

#define LAS __attribute__((address_space(3)))
#define GAS __attribute__((address_space(1)))
typedef unsigned short u16;
typedef short bf16x8 __attribute__((ext_vector_type(8)));
typedef short bf16x4 __attribute__((ext_vector_type(4)));
typedef float f32x4 __attribute__((ext_vector_type(4)));
typedef float f32x2 __attribute__((ext_vector_type(2)));
typedef unsigned u32x4 __attribute__((ext_vector_type(4)));
typedef unsigned u32x2 __attribute__((ext_vector_type(2)));
typedef __bf16 bf16x2_t __attribute__((ext_vector_type(2)));
typedef unsigned long long u64;
typedef GAS u64 gu64;

constexpr int SEQ = 2048, DM = 1024, FF = 3584;

__device__ __forceinline__ unsigned pack_bf16(float lo, float hi) {
    f32x2 v = {lo, hi};
    return __builtin_bit_cast(unsigned, __builtin_convertvector(v, bf16x2_t));
}
__device__ __forceinline__ float bf_lo(unsigned u) { return __uint_as_float(u << 16); }
__device__ __forceinline__ float bf_hi(unsigned u) { return __uint_as_float(u & 0xffff0000u); }
__device__ __forceinline__ float bf_round(float f) { return bf_lo(pack_bf16(f, f)); }

__device__ __forceinline__ float wave_sum(float v) {
    v += __builtin_bit_cast(float, __builtin_amdgcn_update_dpp(0, __builtin_bit_cast(int, v), 0x111, 0xf, 0xf, true));
    v += __builtin_bit_cast(float, __builtin_amdgcn_update_dpp(0, __builtin_bit_cast(int, v), 0x112, 0xf, 0xf, true));
    v += __builtin_bit_cast(float, __builtin_amdgcn_update_dpp(0, __builtin_bit_cast(int, v), 0x114, 0xf, 0xf, true));
    v += __builtin_bit_cast(float, __builtin_amdgcn_update_dpp(0, __builtin_bit_cast(int, v), 0x118, 0xf, 0xf, true));
    v += __builtin_bit_cast(float, __builtin_amdgcn_update_dpp(0, __builtin_bit_cast(int, v), 0x142, 0xa, 0xf, true));
    v += __builtin_bit_cast(float, __builtin_amdgcn_update_dpp(0, __builtin_bit_cast(int, v), 0x143, 0xc, 0xf, true));
    return __builtin_bit_cast(float, __builtin_amdgcn_readlane(__builtin_bit_cast(int, v), 63));
}
typedef __amdgpu_buffer_rsrc_t rsrc_t;
__device__ __forceinline__ rsrc_t mk_rsrc(const void* base, unsigned bytes) { return __builtin_amdgcn_make_buffer_rsrc((void*)base, 0, bytes, 0x00020000); }
__device__ __forceinline__ void st16(rsrc_t r, unsigned off, u32x4 v) { __builtin_amdgcn_raw_buffer_store_b128(v, r, off, 0, 16); }
__device__ __forceinline__ void st8(rsrc_t r, unsigned off, u32x2 v) { __builtin_amdgcn_raw_buffer_store_b64(v, r, off, 0, 16); }
__device__ __forceinline__ u32x4 ld16_stream(const void* p) { return __builtin_nontemporal_load((const u32x4*)p); }

#define WAIT_V(n) asm volatile("s_waitcnt vmcnt(" #n ")" ::: "memory")
#define WAIT_L(n) asm volatile("s_waitcnt lgkmcnt(" #n ")" ::: "memory")
#define BAR() __builtin_amdgcn_s_barrier()
#define BARM() do { asm volatile("" ::: "memory"); __builtin_amdgcn_s_barrier(); asm volatile("" ::: "memory"); } while (0)
#define SCHED() __builtin_amdgcn_sched_barrier(0)
#define GLDS(gptr, ldsptr) __builtin_amdgcn_global_load_lds((const GAS unsigned*)(gptr), (LAS unsigned*)(ldsptr), 16, 0, 0)
#define GLDS_DEV(gptr, ldsptr) __builtin_amdgcn_global_load_lds((const GAS unsigned*)(gptr), (LAS unsigned*)(ldsptr), 16, 0, 16)
#define TRRD(dst, addr, off) asm volatile("ds_read_b64_tr_b16 %0, %1 offset:%c2" : "=&v"(dst) : "v"(addr), "i"(off) : "memory")
#define RD128(dst, addr, off) asm volatile("ds_read_b128 %0, %1 offset:%c2" : "=&v"(dst) : "v"(addr), "i"(off) : "memory")
#define CAT(lo, hi) __builtin_shufflevector(lo, hi, 0, 1, 2, 3, 4, 5, 6, 7)

struct Params {
    const u16 *x, *kin, *vin, *shk, *ssmk, *ssmD, *wg, *wu, *wd;
    u16 *out, *xn, *kvT, *y2T, *hbuf;
    f32x2* stats;
    float* part;
    u64* sync;
};
constexpr int SY_EPOCH = 0, SY_X = 128, SY_K = 512, SY_H = 1024, SY_Y = 1536, SY_P = 2048;

__device__ __forceinline__ void signal(u64* word, u64 tag) { __hip_atomic_store((gu64*)word, tag, __ATOMIC_RELAXED, __HIP_MEMORY_SCOPE_AGENT); }
__device__ __forceinline__ void poll_words(const u64* word, bool want, u64 tag) {
    const u64 t0 = __builtin_amdgcn_s_memrealtime();
    for (;;) {
        const u64 v = want ? __hip_atomic_load((gu64*)word, __ATOMIC_RELAXED, __HIP_MEMORY_SCOPE_AGENT) : tag;
        if (__all(v == tag)) break;
        if (__builtin_amdgcn_s_memrealtime() - t0 > 5000000ull) break;
        __builtin_amdgcn_s_sleep(2);
    }
    asm volatile("" ::: "memory");
}
__device__ __forceinline__ bool last_wave(LAS unsigned* cnt, unsigned n) {
    unsigned old = 0; unsigned long long ex; const unsigned addr = (unsigned)(uintptr_t)cnt, one = 1u;
    asm volatile("s_mov_b64 %1, exec\n\ts_mov_b64 exec, 1\n\tds_add_rtn_u32 %0, %2, %3\n\ts_waitcnt lgkmcnt(0)\n\ts_mov_b64 exec, %1" : "+v"(old), "=&s"(ex) : "v"(addr), "v"(one) : "memory");
    return (unsigned)__builtin_amdgcn_readfirstlane(old) == n - 1;
}

constexpr int CV_XB0 = 512, CV_XB1 = 4608 + 512, CV_HE = 9216, CV_HO = 9216 + 4160, CV_WAVE = 20480;
struct ConvPre { u32x4 nh[4]; };
__device__ __forceinline__ void conv_prefetch(const Params& P, int cb, ConvPre& pre) {
    const int wid = __builtin_amdgcn_readfirstlane(threadIdx.x >> 6), lane = threadIdx.x & 63;
#pragma unroll
    for (int i = 0; i < 4; ++i) { const u16* q = P.ssmk + (size_t)(cb * 32 + wid * 4) * SEQ + (i * 64 + lane) * 8; asm volatile("global_load_dwordx4 %0, %1, off nt" : "=&v"(pre.nh[i]) : "v"(q) : "memory"); }
}
__device__ __forceinline__ void conv_block(const Params& P, LAS unsigned char* lds, int cb, ConvPre& pre) {
    const int tid = threadIdx.x, c0 = cb * 32;
    const int wid = __builtin_amdgcn_readfirstlane(tid >> 6), lane = tid & 63;
    LAS unsigned char* wl = lds + wid * CV_WAVE;
    const int chw = c0 + wid * 4;
    const rsrc_t rkv = mk_rsrc(P.kvT, SEQ * DM * 2); const u16* ssmk = P.ssmk;
    u32x4 nx[4], nh[4], no[4];
#define CV_GX(ch) do { _Pragma("unroll") for (int i = 0; i < 4; ++i) nx[i] = __builtin_amdgcn_raw_buffer_load_b128(rkv, (unsigned)(((ch) * SEQ + (i * 64 + lane) * 8) * 2), 0, 16); } while (0)
#define CV_GH(ch) do { _Pragma("unroll") for (int i = 0; i < 4; ++i) nh[i] = ld16_stream(ssmk + (size_t)(ch) * SEQ + (i * 64 + lane) * 8); } while (0)
    CV_GX(chw);
    if (lane < 32) { *(LAS u32x4*)(wl + lane * 16) = (u32x4){0u, 0u, 0u, 0u}; *(LAS u32x4*)(wl + 4608 + lane * 16) = (u32x4){0u, 0u, 0u, 0u}; }
    if (lane < 2) { *(LAS u32x4*)(wl + CV_HE + lane * 16) = (u32x4){0u, 0u, 0u, 0u}; *(LAS u32x4*)(wl + CV_HO + lane * 16) = (u32x4){0u, 0u, 0u, 0u}; }
    const int r = lane & 15, g = lane >> 4, g0 = g & 1, gh = g >> 1;
    const int s0 = r - 8 * g0 + 9 + 16 * gh;
    const int aoff = (r & 1) ? (CV_HE + 2 * s0) : (CV_HO + 2 * (s0 + 1));
    const int boff = 32 * (r - gh) + 16 * g0 - 448;
    const rsrc_t ry = mk_rsrc(P.y2T, SEQ * DM * 2);
    LAS unsigned char* abase[4];
#pragma unroll
    for (int i = 0; i < 4; ++i) { unsigned a = (unsigned)(uintptr_t)(wl + aoff + 1024 * i); asm volatile("" : "+v"(a)); abase[i] = (LAS unsigned char*)(uintptr_t)a; }
    unsigned dmask[4];
#pragma unroll
    for (int i = 0; i < 4; ++i) dmask[i] = (gh == 0 && 8 * g0 + 7 - 2 * i == r) ? 0x0000ffffu : (gh == 0 && 8 * g0 + 6 - 2 * i == r) ? 0xffff0000u : 0u;
    const u32x2 dpair = *(const u32x2*)(P.ssmD + chw);
#define CV_PUTX(par) do { _Pragma("unroll") for (int i = 0; i < 4; ++i) *(LAS u32x4*)(wl + ((par) ? CV_XB1 : CV_XB0) + (i * 64 + lane) * 16) = nx[i]; } while (0)
#define CV_SHIFT() do { unsigned rot[4]; \
        _Pragma("unroll") for (int i = 0; i < 4; ++i) rot[i] = (unsigned)__builtin_amdgcn_update_dpp(0, (int)nh[i][3], 0x13C  , 0xf, 0xf, false); \
        _Pragma("unroll") for (int i = 0; i < 4; ++i) { \
            const unsigned prev = (lane == 0) ? (i == 0 ? 0u : rot[i > 0 ? i - 1 : 0]) : rot[i]; \
            no[i][0] = __builtin_amdgcn_alignbit(nh[i][0], prev, 16); no[i][1] = __builtin_amdgcn_alignbit(nh[i][1], nh[i][0], 16); \
            no[i][2] = __builtin_amdgcn_alignbit(nh[i][2], nh[i][1], 16); no[i][3] = __builtin_amdgcn_alignbit(nh[i][3], nh[i][2], 16); } } while (0)
#define CV_PUTH() do { _Pragma("unroll") for (int i = 0; i < 4; ++i) { \
            *(LAS u32x4*)(wl + CV_HE + 32 + (i * 64 + lane) * 16) = nh[i]; *(LAS u32x4*)(wl + CV_HO + 32 + (i * 64 + lane) * 16) = no[i]; } } while (0)
#define CV_LOAD(buf, par, v) do { _Pragma("unroll") for (int u = 0; u < 8; ++u) { \
            bf[buf][u] = *(const LAS bf16x8*)(wl + ((par) ? CV_XB1 : CV_XB0) + boff + 512 * u + 64 * (7 - (v))); \
            const LAS unsigned* pa = (const LAS unsigned*)(abase[u >> 1] + 64 * (8 * (u & 1) + (v))); \
            u32x4 t; t[0] = pa[0]; t[1] = pa[1]; t[2] = pa[2]; t[3] = pa[3]; \
            af[buf][u] = __builtin_bit_cast(bf16x8, t); } } while (0)
#define CV_EPI(a, ch, c) do { u32x2 o; o[0] = pack_bf16(acc[a][c][0], acc[a][c][1]); o[1] = pack_bf16(acc[a][c][2], acc[a][c][3]); \
            st8(ry, (unsigned)(((ch) * SEQ + 256 * (c) + 16 * r + 4 * g) * 2), o); } while (0)
    bf16x8 af[2][8], bf[2][8];
    f32x4 acc[2][8];
    unsigned touch = 0;
#pragma unroll
    for (int i = 0; i < 4; ++i) nh[i] = pre.nh[i];
    CV_SHIFT(); CV_PUTH();
    CV_PUTX(0);
    CV_LOAD(0, 0, 0);
    CV_GX(chw + 1); CV_GH(chw + 1);
    SCHED();
#pragma unroll
    for (int cc = 0; cc < 4; ++cc) {
        const int a = cc & 1;
        if (cc == 0) __builtin_amdgcn_s_setprio(3); else if (cc == 2) __builtin_amdgcn_s_setprio(2); else if (cc == 3) __builtin_amdgcn_s_setprio(1);
        const unsigned dw = dpair[cc >> 1], dd = (cc & 1) ? ((dw & 0xffff0000u) | (dw >> 16)) : ((dw << 16) | (dw & 0xffffu));
        const u32x4 adw = {dd & dmask[0], dd & dmask[1], dd & dmask[2], dd & dmask[3]};
        const bf16x8 ad = __builtin_bit_cast(bf16x8, adw);
#pragma unroll
        for (int v = 0; v < 8; ++v) {
            const int cur = v & 1, nxt = cur ^ 1;
            if (cc == 3 && v == 4) __builtin_amdgcn_s_setprio(0);
            if (v < 7) CV_LOAD(nxt, a, v + 1);
            else if (cc < 3) { CV_PUTH(); CV_LOAD(nxt, a ^ 1, 0); }
            if (v == 4 && cc < 3) CV_PUTX(a ^ 1);
            if (v == 5 && cc < 3) { CV_SHIFT(); if (cc < 2) CV_GX(chw + cc + 2); }
            if (v == 0 && cc > 0 && cc < 3) CV_GH(chw + cc + 1);
            if (v == 3) {
                const int ln = ((cb * 8 + wid) * 4 + cc) * 64 + lane;
                const char* q = (const char*)P.wd + (size_t)(ln < FF * 16 ? ln : 0) * 128;
                asm volatile("global_load_dword %0, %1, off" : "+v"(touch) : "v"(q) : "memory");
            }
            if (v == 0) {
#pragma unroll
                for (int c = 0; c < 8; ++c) acc[a][c] = __builtin_amdgcn_mfma_f32_16x16x32_bf16(ad, bf[cur][c], (f32x4){0.f, 0.f, 0.f, 0.f}, 0, 0, 0);
            }
#pragma unroll
            for (int u = 0; u < 8; ++u)
#pragma unroll
                for (int c = u; c < 8; ++c) acc[a][c] = __builtin_amdgcn_mfma_f32_16x16x32_bf16(af[cur][u], bf[cur][c - u], acc[a][c], 0, 0, 0);
            if (cc > 0 && v >= 1 && v < 3) {
#pragma unroll
                for (int c = 0; c < 4; ++c) CV_EPI(a ^ 1, chw + cc - 1, 4 * (v - 1) + c);
            }
            if (v == 7) {
                if (cc < 3) {
#pragma unroll
                    for (int i = 0; i < 8; ++i) { __builtin_amdgcn_sched_group_barrier(0x008, 1, 0); __builtin_amdgcn_sched_group_barrier(0x200, 1, 0); }
#pragma unroll
                    for (int i = 0; i < 24; ++i) { __builtin_amdgcn_sched_group_barrier(0x008, 1, 0); __builtin_amdgcn_sched_group_barrier(0x100, 1, 0); }
                }
            } else {
#pragma unroll
                for (int i = 0; i < 24; ++i) { __builtin_amdgcn_sched_group_barrier(0x008, 1, 0); __builtin_amdgcn_sched_group_barrier(0x100, 1, 0); }
                if (v == 0 && cc > 0 && cc < 3) {
#pragma unroll
                    for (int i = 0; i < 4; ++i) { __builtin_amdgcn_sched_group_barrier(0x008, 1, 0); __builtin_amdgcn_sched_group_barrier(0x020, 1, 0); }
                }
                if (cc > 0 && v >= 1 && v < 3) {
#pragma unroll
                    for (int i = 0; i < 4; ++i) { __builtin_amdgcn_sched_group_barrier(0x008, 1, 0); __builtin_amdgcn_sched_group_barrier(0x002, 2, 0); __builtin_amdgcn_sched_group_barrier(0x008, 1, 0); __builtin_amdgcn_sched_group_barrier(0x040, 1, 0); }
                }
                if (v == 4 && cc < 3) {
#pragma unroll
                    for (int i = 0; i < 4; ++i) { __builtin_amdgcn_sched_group_barrier(0x008, 1, 0); __builtin_amdgcn_sched_group_barrier(0x200, 1, 0); }
                }
                if (v == 5 && cc < 3) {
#pragma unroll
                    for (int i = 0; i < 12; ++i) { __builtin_amdgcn_sched_group_barrier(0x008, 1, 0); __builtin_amdgcn_sched_group_barrier(0x002, 2, 0); }
                }
            }
            SCHED();
        }
    }
#pragma unroll
    for (int c = 0; c < 8; ++c) CV_EPI(1, chw + 3, c);
    __builtin_amdgcn_s_setprio(0);
    WAIT_V(0); asm volatile("" :: "v"(touch));
#undef CV_GX
#undef CV_GH
#undef CV_PUTX
#undef CV_SHIFT
#undef CV_PUTH
#undef CV_LOAD
#undef CV_EPI
}

constexpr int KB_STAGE = 32768, KC_N1 = 23, LDS_BYTES = 5 * KB_STAGE;
constexpr int SYNC_CNT = 5 * 32768 - 16;
struct G2Load { unsigned vA1, vA2, vB0, vB1; const char *cA, *cB; };
#define KD_B4(pb, pl) do { GLDS((pb) + g2.vB0, (pl) + 16384 + lw * 4096); GLDS((pb) + g2.vB0 + (size_t)4 * DM * 2, (pl) + 16384 + lw * 4096 + 1024); \
        GLDS((pb) + g2.vB1, (pl) + 16384 + lw * 4096 + 2048); GLDS((pb) + g2.vB1 + (size_t)4 * DM * 2, (pl) + 16384 + lw * 4096 + 3072); } while (0)
#define KD_ISSUE1A(s_, slot) do { const char* _a = g2.cA + (size_t)(KC_N1 * ks + (s_)) * 128 + g2.vA1; LAS unsigned char* _l = lds + (slot) * KB_STAGE; \
        GLDS_DEV(_a, _l + lw * 4096); GLDS_DEV(_a + (size_t)8 * FF * 2, _l + lw * 4096 + 1024); GLDS_DEV(_a + (size_t)16 * FF * 2, _l + lw * 4096 + 2048); GLDS_DEV(_a + (size_t)24 * FF * 2, _l + lw * 4096 + 3072); } while (0)
#define KD_ISSUE1B(s_, slot) do { const char* _b = g2.cB + (size_t)(KC_N1 * ks + (s_)) * 64 * DM * 2; KD_B4(_b, lds + (slot) * KB_STAGE); } while (0)
#define KD_ISSUE1(s_, slot) do { KD_ISSUE1A(s_, slot); KD_ISSUE1B(s_, slot); } while (0)
#define KD_ISSUE2(s_, slot) do { const int _kt = KC_N1 + (s_); const char* _a = g2.cA + (size_t)_kt * 128 + g2.vA2; const char* _b = g2.cB + (size_t)_kt * 64 * DM * 2; LAS unsigned char* _l = lds + (slot) * KB_STAGE; \
        GLDS_DEV(_a, _l + ks * 8192 + lw * 2048); GLDS_DEV(_a + (size_t)8 * FF * 2, _l + ks * 8192 + lw * 2048 + 1024); KD_B4(_b, _l); } while (0)
__device__ __forceinline__ G2Load gemm2_addr(const Params& P, int ks, int pm, int pn, int lw, int lane) {
    G2Load g2;
    const unsigned swA = (unsigned)(8 * ((lane & 7) ^ ((lane >> 3) & 7)) * 2);
    g2.vA1 = (unsigned)((lw * 32 + (lane >> 3)) * FF * 2) + swA;
    g2.vA2 = (unsigned)((ks * 64 + lw * 16 + (lane >> 3)) * FF * 2) + swA;
    g2.vB0 = (unsigned)(((lw * 16 + (lane >> 4)) * DM + 8 * ((lane & 15) ^ (((lane >> 4) & 3) << 2))) * 2);
    g2.vB1 = (unsigned)(((lw * 16 + 8 + (lane >> 4)) * DM + 8 * ((lane & 15) ^ ((((lane >> 4) & 3) << 2) | 2))) * 2);
    g2.cA = (const char*)P.hbuf + (size_t)pm * 128 * FF * 2;
    g2.cB = (const char*)P.wd + (size_t)pn * 128 * 2;
    return g2;
}

constexpr int HTB = 16384;
constexpr int PA_K = 0, PA_V = 17408, PA_T = 34816, PA_C = 51200;
#define SA(b, h) (((b) * 2 + (h)) * HTB)
#define SB(b, h) ((4 + (b) * 2 + (h)) * HTB)
struct G1Addr { unsigned voffA, voffB; const char *cA, *cG, *cU; unsigned ldsw; };
#define STAGE_A(bufoff, h, kt) do { const char* _g = ga.cA + (h) * ((size_t)128 * DM * 2) + (size_t)(kt) * 128; \
        GLDS_DEV(_g + ga.voffA, lds + (bufoff) + ga.ldsw); GLDS_DEV(_g + (size_t)64 * DM * 2 + ga.voffA, lds + (bufoff) + ga.ldsw + 8192); } while (0)
#define STAGE_B(bufoff, base, kt) do { const char* _g = (base) + (size_t)(kt) * ((size_t)64 * FF * 2); \
        GLDS(_g + ga.voffB, lds + (bufoff) + ga.ldsw); GLDS(_g + (size_t)32 * FF * 2 + ga.voffB, lds + (bufoff) + ga.ldsw + 8192); } while (0)
__device__ __forceinline__ G1Addr gemm1_addr(const Params& P, int pm, int pn) {
    const int tid = threadIdx.x, wid = __builtin_amdgcn_readfirstlane(tid >> 6);
    G1Addr ga;
    ga.voffA = (unsigned)(((tid >> 3) * DM + 8 * ((tid & 7) ^ ((tid >> 3) & 7))) * 2);
    const int kb = tid >> 4;
    ga.voffB = (unsigned)((kb * FF + 8 * ((tid & 15) ^ (((kb & 3) << 2) | (((kb >> 3) & 1) << 1)))) * 2);
    ga.cA = (const char*)P.xn + (size_t)pm * 256 * DM * 2;
    ga.cG = (const char*)P.wg + (size_t)pn * 128 * 2;
    ga.cU = (const char*)P.wu + (size_t)pn * 128 * 2;
    ga.ldsw = (unsigned)wid * 1024u;
    return ga;
}
__device__ __forceinline__ void gemm1_block(const Params& P, LAS unsigned char* lds, const G1Addr& ga, const G2Load& g2, int ks, int pm, int pn, u64 tag) {
    const int tid = threadIdx.x, wid = __builtin_amdgcn_readfirstlane(tid >> 6), lane = tid & 63, wr = wid >> 2, wc = wid & 3, fr = lane & 15, fq = lane >> 4;
    constexpr int nt = DM / 64;
    const int q = (lane & 15) >> 2, p = lane & 3;
    const int aoff0 = (wr * 64 + fr) * 128 + 16 * ((fq) ^ (fr & 7)), aoff1 = (wr * 64 + fr) * 128 + 16 * ((4 + fq) ^ (fr & 7));
    const int boff0 = 256 * (8 * fq + q) + 16 * (4 * (wc ^ q) + 2 * (0 ^ (fq & 1)) + (p >> 1)) + 8 * (p & 1);
    const int boff1 = 256 * (8 * fq + q) + 16 * (4 * (wc ^ q) + 2 * (1 ^ (fq & 1)) + (p >> 1)) + 8 * (p & 1);
#define LDA(dst, b, h) do { _Pragma("unroll") for (int m = 0; m < 4; ++m) { \
        dst[m][0] = *(const LAS bf16x8*)(lds + SA(b, h) + aoff0 + m * 2048); dst[m][1] = *(const LAS bf16x8*)(lds + SA(b, h) + aoff1 + m * 2048); } } while (0)
#define LDB(dst, b, h) do { \
        TRRD(dst[0][0][0], bb0, SB(b, h) - SB(0, 0)); TRRD(dst[0][0][1], bb0, SB(b, h) - SB(0, 0) + 1024); TRRD(dst[1][0][0], bb1, SB(b, h) - SB(0, 0)); TRRD(dst[1][0][1], bb1, SB(b, h) - SB(0, 0) + 1024); \
        TRRD(dst[0][1][0], bb0, SB(b, h) - SB(0, 0) + 8192); TRRD(dst[0][1][1], bb0, SB(b, h) - SB(0, 0) + 9216); TRRD(dst[1][1][0], bb1, SB(b, h) - SB(0, 0) + 8192); TRRD(dst[1][1][1], bb1, SB(b, h) - SB(0, 0) + 9216); } while (0)
#define WAIT_LB(dst) asm volatile("s_waitcnt lgkmcnt(0)" : "+v"(dst[0][0][0]), "+v"(dst[0][0][1]), "+v"(dst[0][1][0]), "+v"(dst[0][1][1]), "+v"(dst[1][0][0]), "+v"(dst[1][0][1]), "+v"(dst[1][1][0]), "+v"(dst[1][1][1]) :: "memory")
#define MMA(ai, bj, At, Bt) do { __builtin_amdgcn_s_setprio(1); _Pragma("unroll") for (int m = 0; m < 4; ++m) _Pragma("unroll") for (int n = 0; n < 2; ++n) _Pragma("unroll") for (int k = 0; k < 2; ++k) \
        acc[ai][bj][m][n] = __builtin_amdgcn_mfma_f32_16x16x32_bf16(CAT(Bt[n][k][0], Bt[n][k][1]), At[m][k], acc[ai][bj][m][n], 0, 0, 0); __builtin_amdgcn_s_setprio(0); } while (0)
    f32x4 acc[2][2][4][2];
#pragma unroll
    for (int a = 0; a < 2; ++a)
#pragma unroll
        for (int b = 0; b < 2; ++b)
#pragma unroll
            for (int m = 0; m < 4; ++m)
#pragma unroll
                for (int n = 0; n < 2; ++n) acc[a][b][m][n] = (f32x4){0.f, 0.f, 0.f, 0.f};
    bf16x8 At[4][2]; bf16x4 B0[2][2][2], B1[2][2][2];
    const unsigned bb0 = (unsigned)(uintptr_t)(lds + SB(0, 0) + boff0), bb1 = (unsigned)(uintptr_t)(lds + SB(0, 0) + boff1);
    if (wr == 1) BAR();
    WAIT_V(2); WAIT_L(0); BAR(); BAR();
    for (int t = 0; t < nt - 2; t += 2) {
        LDB(B0, 0, 0); SCHED(); LDA(At, 0, 0); STAGE_A(SA(1, 1), 1, t + 1);
        WAIT_L(8); BAR(); WAIT_LB(B0); SCHED(); MMA(0, 0, At, B0); BAR(); SCHED();
        LDB(B1, 0, 1); STAGE_B(SB(0, 0), ga.cG, t + 2);
        BAR(); WAIT_LB(B1); SCHED(); MMA(0, 1, At, B1); BAR();
        LDA(At, 0, 1); STAGE_A(SA(0, 0), 0, t + 2);
        BAR(); WAIT_L(0); MMA(1, 0, At, B0); BAR(); SCHED();
        STAGE_B(SB(0, 1), ga.cU, t + 2);
        WAIT_V(6);
        BAR(); MMA(1, 1, At, B1); BAR();
        LDB(B0, 1, 0); SCHED(); LDA(At, 1, 0); STAGE_A(SA(0, 1), 1, t + 2);
        WAIT_L(8); BAR(); WAIT_LB(B0); SCHED(); MMA(0, 0, At, B0); BAR(); SCHED();
        LDB(B1, 1, 1); STAGE_B(SB(1, 0), ga.cG, t + 3);
        BAR(); WAIT_LB(B1); SCHED(); MMA(0, 1, At, B1); BAR();
        LDA(At, 1, 1); STAGE_A(SA(1, 0), 0, t + 3);
        BAR(); WAIT_L(0); MMA(1, 0, At, B0); BAR(); SCHED();
        STAGE_B(SB(1, 1), ga.cU, t + 3);
        WAIT_V(6); BAR(); MMA(1, 1, At, B1); BAR();
    }
    int tz = threadIdx.x; asm volatile("" : "+v"(tz));
    const int el = tz & 63, efr = el & 15, efq = el >> 4;
    const unsigned hl = (unsigned)(uintptr_t)lds + wr * KB_STAGE + efr * 256 + 16 * ((wc * 4 + 2 * (efq & 1) + (efq >> 1)) ^ efr);
#define EPI(ai, m) do { unsigned pk[2][2]; \
        _Pragma("unroll") for (int n = 0; n < 2; ++n) _Pragma("unroll") for (int j = 0; j < 2; ++j) { \
            const f32x2 gs = (f32x2){acc[ai][0][m][n][2 * j], acc[ai][0][m][n][2 * j + 1]} * -1.4426950408889634f; \
            const f32x2 d = (f32x2){__builtin_amdgcn_exp2f(gs[0]), __builtin_amdgcn_exp2f(gs[1])} + 1.0f; \
            const f32x2 hv = (f32x2){acc[ai][1][m][n][2 * j], acc[ai][1][m][n][2 * j + 1]} * (f32x2){__builtin_amdgcn_rcpf(d[0]), __builtin_amdgcn_rcpf(d[1])}; \
            pk[n][j] = pack_bf16(hv[0], hv[1]); } \
        const auto s0 = __builtin_amdgcn_permlane16_swap(pk[0][0], pk[1][0], false, false); \
        const auto s1 = __builtin_amdgcn_permlane16_swap(pk[0][1], pk[1][1], false, false); \
        const u32x4 w = {s0[0], s1[0], s0[1], s1[1]}; \
        asm volatile("ds_write_b128 %0, %1 offset:%c2" :: "v"(hl + (ai) * 2 * KB_STAGE), "v"(w), "i"((m) * 4096) : "memory"); } while (0)
    const rsrc_t rh = mk_rsrc(P.hbuf, SEQ * FF * 2);
#define PUTH(ai) do { u32x4 w[4]; \
        _Pragma("unroll") for (int i = 0; i < 4; ++i) { const int row = 128 * (ai) + 16 * wid + 4 * i + efq; \
            RD128(w[i], (unsigned)(uintptr_t)lds + (row >> 6) * KB_STAGE + (row & 63) * 256 + 16 * (efr ^ (row & 15)), 0); } \
        asm volatile("s_waitcnt lgkmcnt(0)" : "+v"(w[0]), "+v"(w[1]), "+v"(w[2]), "+v"(w[3]) :: "memory"); \
        _Pragma("unroll") for (int i = 0; i < 4; ++i) st16(rh, (unsigned)(((pm * 256 + 128 * (ai) + 16 * wid + 4 * i + efq) * FF + pn * 128 + 8 * efr) * 2), w[i]); } while (0)
    {
        LDB(B0, 0, 0); SCHED(); LDA(At, 0, 0); STAGE_A(SA(1, 1), 1, nt - 1);
        WAIT_L(8); BAR(); WAIT_LB(B0); SCHED(); MMA(0, 0, At, B0); BAR(); SCHED();
        LDB(B1, 0, 1); BAR(); WAIT_LB(B1); SCHED(); MMA(0, 1, At, B1); BAR();
        LDA(At, 0, 1); BAR(); WAIT_L(0); MMA(1, 0, At, B0); BAR(); SCHED();
        WAIT_V(0); BAR(); MMA(1, 1, At, B1); BAR();
    }
    {
        LDB(B0, 1, 0); SCHED(); LDA(At, 1, 0); BAR(); WAIT_LB(B0); SCHED(); MMA(0, 0, At, B0); BAR(); SCHED();
        LDB(B1, 1, 1); BAR(); WAIT_LB(B1); SCHED(); MMA(0, 1, At, B1); BAR();
        LDA(At, 1, 1); BAR(); WAIT_L(0); MMA(1, 0, At, B0); MMA(1, 1, At, B1);
    }
    if (tid == 0) *(LAS unsigned*)(lds + SYNC_CNT) = 0u;
    if (wr == 0) BAR();
    WAIT_L(0); BAR();
    if (wid >= 4) { const int lw = wid - 4; KD_ISSUE1B(0, 0); KD_ISSUE1B(1, 1); KD_ISSUE1B(2, 2); KD_ISSUE1B(3, 3); }
    EPI(0, 0); EPI(0, 1); EPI(0, 2); EPI(0, 3);
    WAIT_L(0); BAR();
    PUTH(0);
    EPI(1, 0); EPI(1, 1); EPI(1, 2); EPI(1, 3);
    WAIT_L(0); BAR();
    PUTH(1);
#undef EPI
#undef PUTH
#undef LDA
#undef LDB
#undef WAIT_LB
#undef MMA
}

#define KE_S01(XA, XB, YA, YB, ya0, ya1, yc0, yc1, yb0, yb1, yb2, yb3) do { \
        TRRD(YB[0][0][0], yb0, 0); TRRD(YB[0][0][1], yb0, 1024); RD128(YA[0][0], ya0, 0); TRRD(YB[0][1][0], yb1, 0); TRRD(YB[0][1][1], yb1, 1024); RD128(YA[0][1], ya0, 2048); TRRD(YB[0][2][0], yb2, 0); TRRD(YB[0][2][1], yb2, 1024); RD128(YA[0][2], yc0, 4096); TRRD(YB[0][3][0], yb3, 0); TRRD(YB[0][3][1], yb3, 1024); RD128(YA[0][3], yc0, 6144); TRRD(YB[1][0][0], yb0, 8192); TRRD(YB[1][0][1], yb0, 9216); RD128(YA[1][0], ya1, 0); TRRD(YB[1][1][0], yb1, 8192); TRRD(YB[1][1][1], yb1, 9216); RD128(YA[1][1], ya1, 2048); TRRD(YB[1][2][0], yb2, 8192); TRRD(YB[1][2][1], yb2, 9216); RD128(YA[1][2], yc1, 4096); TRRD(YB[1][3][0], yb3, 8192); TRRD(YB[1][3][1], yb3, 9216); RD128(YA[1][3], yc1, 6144); SCHED(); } while (0)
#define KE_S11(XA, XB, YA, YB, ya0, ya1, yc0, yc1, yb0, yb1, yb2, yb3) do { \
        KE_M(XA, XB, 0, 0, 0); SCHED(); TRRD(YB[0][0][0], yb0, 0); SCHED(); \
        KE_M(XA, XB, 0, 0, 1); SCHED(); TRRD(YB[0][0][1], yb0, 1024); SCHED(); \
        KE_M(XA, XB, 0, 0, 2); SCHED(); RD128(YA[0][0], ya0, 0); SCHED(); \
        KE_M(XA, XB, 0, 0, 3); SCHED(); TRRD(YB[0][1][0], yb1, 0); SCHED(); \
        KE_M(XA, XB, 0, 1, 0); SCHED(); TRRD(YB[0][1][1], yb1, 1024); SCHED(); \
        KE_M(XA, XB, 0, 1, 1); SCHED(); RD128(YA[0][1], ya0, 2048); SCHED(); \
        KE_M(XA, XB, 0, 1, 2); SCHED(); TRRD(YB[0][2][0], yb2, 0); SCHED(); \
        KE_M(XA, XB, 0, 1, 3); SCHED(); TRRD(YB[0][2][1], yb2, 1024); SCHED(); \
        KE_M(XA, XB, 0, 2, 0); SCHED(); RD128(YA[0][2], yc0, 4096); SCHED(); \
        KE_M(XA, XB, 0, 2, 1); SCHED(); TRRD(YB[0][3][0], yb3, 0); SCHED(); \
        KE_M(XA, XB, 0, 2, 2); SCHED(); TRRD(YB[0][3][1], yb3, 1024); SCHED(); \
        KE_M(XA, XB, 0, 2, 3); SCHED(); RD128(YA[0][3], yc0, 6144); SCHED(); \
        KE_M(XA, XB, 0, 3, 0); SCHED(); TRRD(YB[1][0][0], yb0, 8192); SCHED(); \
        KE_M(XA, XB, 0, 3, 1); SCHED(); TRRD(YB[1][0][1], yb0, 9216); SCHED(); \
        KE_M(XA, XB, 0, 3, 2); SCHED(); RD128(YA[1][0], ya1, 0); SCHED(); \
        KE_M(XA, XB, 0, 3, 3); SCHED(); TRRD(YB[1][1][0], yb1, 8192); SCHED(); \
        KE_M(XA, XB, 1, 0, 0); SCHED(); TRRD(YB[1][1][1], yb1, 9216); SCHED(); \
        KE_M(XA, XB, 1, 0, 1); SCHED(); RD128(YA[1][1], ya1, 2048); SCHED(); \
        KE_M(XA, XB, 1, 0, 2); SCHED(); TRRD(YB[1][2][0], yb2, 8192); SCHED(); \
        KE_M(XA, XB, 1, 0, 3); SCHED(); TRRD(YB[1][2][1], yb2, 9216); SCHED(); \
        KE_M(XA, XB, 1, 1, 0); SCHED(); RD128(YA[1][2], yc1, 4096); SCHED(); \
        KE_M(XA, XB, 1, 1, 1); SCHED(); TRRD(YB[1][3][0], yb3, 8192); SCHED(); \
        KE_M(XA, XB, 1, 1, 2); SCHED(); TRRD(YB[1][3][1], yb3, 9216); SCHED(); \
        KE_M(XA, XB, 1, 1, 3); SCHED(); RD128(YA[1][3], yc1, 6144); SCHED(); \
        KE_M(XA, XB, 1, 2, 0); SCHED(); SCHED(); \
        KE_M(XA, XB, 1, 2, 1); SCHED(); SCHED(); \
        KE_M(XA, XB, 1, 2, 2); SCHED(); SCHED(); \
        KE_M(XA, XB, 1, 2, 3); SCHED(); SCHED(); \
        KE_M(XA, XB, 1, 3, 0); SCHED(); SCHED(); \
        KE_M(XA, XB, 1, 3, 1); SCHED(); SCHED(); \
        KE_M(XA, XB, 1, 3, 2); SCHED(); SCHED(); \
        KE_M(XA, XB, 1, 3, 3); SCHED(); SCHED(); } while (0)
#define KE_S12(XA, XB, YA, YB, ya0, ya1, yc0, yc1, yb0, yb1, yb2, yb3) do { \
        KE_M(XA, XB, 0, 0, 0); SCHED(); TRRD(YB[0][0][0], yb0, 0); SCHED(); \
        KE_M(XA, XB, 0, 0, 1); SCHED(); TRRD(YB[0][0][1], yb0, 1024); SCHED(); \
        KE_M(XA, XB, 0, 0, 2); SCHED(); RD128(YA[0][0], ya0, 0); SCHED(); \
        KE_M(XA, XB, 0, 0, 3); SCHED(); TRRD(YB[0][1][0], yb1, 0); SCHED(); \
        KE_M(XA, XB, 0, 1, 0); SCHED(); TRRD(YB[0][1][1], yb1, 1024); SCHED(); \
        KE_M(XA, XB, 0, 1, 1); SCHED(); RD128(YA[0][1], ya0, 2048); SCHED(); \
        KE_M(XA, XB, 0, 1, 2); SCHED(); TRRD(YB[0][2][0], yb2, 0); SCHED(); \
        KE_M(XA, XB, 0, 1, 3); SCHED(); TRRD(YB[0][2][1], yb2, 1024); SCHED(); \
        KE_M(XA, XB, 0, 2, 0); SCHED(); TRRD(YB[0][3][0], yb3, 0); SCHED(); \
        KE_M(XA, XB, 0, 2, 1); SCHED(); TRRD(YB[0][3][1], yb3, 1024); SCHED(); \
        KE_M(XA, XB, 0, 2, 2); SCHED(); TRRD(YB[1][0][0], yb0, 8192); SCHED(); \
        KE_M(XA, XB, 0, 2, 3); SCHED(); TRRD(YB[1][0][1], yb0, 9216); SCHED(); \
        KE_M(XA, XB, 0, 3, 0); SCHED(); RD128(YA[1][0], ya1, 0); SCHED(); \
        KE_M(XA, XB, 0, 3, 1); SCHED(); TRRD(YB[1][1][0], yb1, 8192); SCHED(); \
        KE_M(XA, XB, 0, 3, 2); SCHED(); TRRD(YB[1][1][1], yb1, 9216); SCHED(); \
        KE_M(XA, XB, 0, 3, 3); SCHED(); RD128(YA[1][1], ya1, 2048); SCHED(); \
        KE_M(XA, XB, 1, 0, 0); SCHED(); TRRD(YB[1][2][0], yb2, 8192); SCHED(); \
        KE_M(XA, XB, 1, 0, 1); SCHED(); TRRD(YB[1][2][1], yb2, 9216); SCHED(); \
        KE_M(XA, XB, 1, 0, 2); SCHED(); TRRD(YB[1][3][0], yb3, 8192); SCHED(); \
        KE_M(XA, XB, 1, 0, 3); SCHED(); TRRD(YB[1][3][1], yb3, 9216); SCHED(); \
        KE_M(XA, XB, 1, 1, 0); SCHED(); SCHED(); \
        KE_M(XA, XB, 1, 1, 1); SCHED(); SCHED(); \
        KE_M(XA, XB, 1, 1, 2); SCHED(); SCHED(); \
        KE_M(XA, XB, 1, 1, 3); SCHED(); SCHED(); \
        KE_M(XA, XB, 1, 2, 0); SCHED(); SCHED(); \
        KE_M(XA, XB, 1, 2, 1); SCHED(); SCHED(); \
        KE_M(XA, XB, 1, 2, 2); SCHED(); SCHED(); \
        KE_M(XA, XB, 1, 2, 3); SCHED(); SCHED(); \
        KE_M(XA, XB, 1, 3, 0); SCHED(); SCHED(); \
        KE_M(XA, XB, 1, 3, 1); SCHED(); SCHED(); \
        KE_M(XA, XB, 1, 3, 2); SCHED(); SCHED(); \
        KE_M(XA, XB, 1, 3, 3); SCHED(); SCHED(); } while (0)
#define KE_S22(XA, XB, YA, YB, ya0, ya1, yc0, yc1, yb0, yb1, yb2, yb3) do { \
        KE_M(XA, XB, 0, 0, 0); SCHED(); TRRD(YB[0][0][0], yb0, 0); TRRD(YB[0][0][1], yb0, 1024); SCHED(); \
        KE_M(XA, XB, 0, 0, 1); SCHED(); RD128(YA[0][0], ya0, 0); TRRD(YB[0][1][0], yb1, 0); SCHED(); \
        KE_M(XA, XB, 0, 0, 2); SCHED(); TRRD(YB[0][1][1], yb1, 1024); RD128(YA[0][1], ya0, 2048); SCHED(); \
        KE_M(XA, XB, 0, 0, 3); SCHED(); TRRD(YB[0][2][0], yb2, 0); TRRD(YB[0][2][1], yb2, 1024); SCHED(); \
        KE_M(XA, XB, 0, 1, 0); SCHED(); TRRD(YB[0][3][0], yb3, 0); SCHED(); \
        KE_M(XA, XB, 0, 1, 1); SCHED(); TRRD(YB[0][3][1], yb3, 1024); SCHED(); \
        KE_M(XA, XB, 0, 1, 2); SCHED(); TRRD(YB[1][0][0], yb0, 8192); SCHED(); \
        KE_M(XA, XB, 0, 1, 3); SCHED(); TRRD(YB[1][0][1], yb0, 9216); SCHED(); \
        KE_M(XA, XB, 1, 0, 0); SCHED(); RD128(YA[1][0], ya1, 0); SCHED(); \
        KE_M(XA, XB, 1, 0, 1); SCHED(); TRRD(YB[1][1][0], yb1, 8192); SCHED(); \
        KE_M(XA, XB, 1, 0, 2); SCHED(); TRRD(YB[1][1][1], yb1, 9216); SCHED(); \
        KE_M(XA, XB, 1, 0, 3); SCHED(); RD128(YA[1][1], ya1, 2048); SCHED(); \
        KE_M(XA, XB, 1, 1, 0); SCHED(); TRRD(YB[1][2][0], yb2, 8192); SCHED(); \
        KE_M(XA, XB, 1, 1, 1); SCHED(); TRRD(YB[1][2][1], yb2, 9216); SCHED(); \
        KE_M(XA, XB, 1, 1, 2); SCHED(); TRRD(YB[1][3][0], yb3, 8192); SCHED(); \
        KE_M(XA, XB, 1, 1, 3); SCHED(); TRRD(YB[1][3][1], yb3, 9216); SCHED(); } while (0)
#define KE_S20(XA, XB, YA, YB, ya0, ya1, yc0, yc1, yb0, yb1, yb2, yb3) do { \
        KE_M(XA, XB, 0, 0, 0); SCHED(); SCHED(); \
        KE_M(XA, XB, 0, 0, 1); SCHED(); SCHED(); \
        KE_M(XA, XB, 0, 0, 2); SCHED(); SCHED(); \
        KE_M(XA, XB, 0, 0, 3); SCHED(); SCHED(); \
        KE_M(XA, XB, 0, 1, 0); SCHED(); SCHED(); \
        KE_M(XA, XB, 0, 1, 1); SCHED(); SCHED(); \
        KE_M(XA, XB, 0, 1, 2); SCHED(); SCHED(); \
        KE_M(XA, XB, 0, 1, 3); SCHED(); SCHED(); \
        KE_M(XA, XB, 1, 0, 0); SCHED(); SCHED(); \
        KE_M(XA, XB, 1, 0, 1); SCHED(); SCHED(); \
        KE_M(XA, XB, 1, 0, 2); SCHED(); SCHED(); \
        KE_M(XA, XB, 1, 0, 3); SCHED(); SCHED(); \
        KE_M(XA, XB, 1, 1, 0); SCHED(); SCHED(); \
        KE_M(XA, XB, 1, 1, 1); SCHED(); SCHED(); \
        KE_M(XA, XB, 1, 1, 2); SCHED(); SCHED(); \
        KE_M(XA, XB, 1, 1, 3); SCHED(); SCHED(); } while (0)
struct G2Epi { u32x4 ex[4]; unsigned es; };
__device__ __forceinline__ void gemm2_epi_request(const Params& P, G2Epi& ep, int ks, int pm, int pn, int lw, int lane) {
#pragma unroll
    for (int i = 0; i < 4; ++i) {
        const int xr = (lw * 4 + i) * 4 + (lane >> 4), xc = (lane & 15) ^ (xr & 15);
        const char* q = (const char*)P.x + ((size_t)(pm * 128 + ks * 64 + xr) * DM + pn * 128 + 8 * xc) * 2;
        asm volatile("global_load_dwordx4 %0, %1, off" : "=&v"(ep.ex[i]) : "v"(q) : "memory");
    }
    const char* q = (const char*)P.stats + (size_t)pm * 128 * 8 + (lw * 64 + lane) * 4;
    asm volatile("global_load_dword %0, %1, off sc1" : "=&v"(ep.es) : "v"(q) : "memory");
}
__device__ __forceinline__ void gemm2_loader(const Params& P, LAS unsigned char* lds, const G2Load& g2, G2Epi& ep, int ks, int pm, int pn, int lw, int lane, u64 tag) {
    const int spare = (1 - ks) * 8192;
    u32x4 ey[4]; u32x4 (&ex)[4] = ep.ex; unsigned& es = ep.es;
#define KD_LDY(i) do { const int row = (lw * 4 + (i)) * 8 + (lane >> 3), ck = (lane & 7) ^ (((row >> 1) & 3) << 1); \
        const char* _p = (const char*)P.y2T + ((size_t)(pn * 128 + row) * SEQ + pm * 128 + ks * 64 + 8 * ck) * 2; \
        asm volatile("global_load_dwordx4 %0, %1, off sc1" : "=&v"(ey[i]) : "v"(_p) : "memory"); } while (0)
    u64 yw = tag;
    const u64* yword = P.sync + SY_Y + 4 * pn + (lane & 3);
#define KD_YASK() asm volatile("global_load_dwordx2 %0, %1, off sc1" : "=&v"(yw) : "v"(yword) : "memory")
#define KD_YCHECK() do { asm volatile("" : "+v"(yw)); if (!__all(yw == tag)) poll_words(yword, true, tag); asm volatile("" ::: "memory"); } while (0)
#define KD_PUT() do { asm volatile("" : "+v"(ey[0]), "+v"(ey[1]), "+v"(ey[2]), "+v"(ey[3]), "+v"(ex[0]), "+v"(ex[1]), "+v"(ex[2]), "+v"(ex[3]), "+v"(es)); \
        _Pragma("unroll") for (int i = 0; i < 4; ++i) { \
            *(LAS u32x4*)(lds + (2 + (lw >> 1)) * KB_STAGE + spare + ((lw & 1) * 4 + i) * 1024 + lane * 16) = ey[i]; \
            *(LAS u32x4*)(lds + (lw >> 1) * KB_STAGE + spare + ((lw & 1) * 4 + i) * 1024 + lane * 16) = ex[i]; } \
        *(LAS unsigned*)(lds + 4 * KB_STAGE + spare + 1024 + (lw * 64 + lane) * 4) = es; } while (0)
        KD_ISSUE1A(0, 0); KD_ISSUE1A(1, 1); KD_ISSUE1A(2, 2); KD_ISSUE1A(3, 3);
        WAIT_V(12); BAR();
        WAIT_V(8); BAR(); KD_ISSUE1(4, 4);
        WAIT_V(12); BAR(); KD_ISSUE1(5, 0);
        WAIT_V(16); BAR(); KD_ISSUE1(6, 1);
        WAIT_V(16); BAR(); KD_ISSUE1(7, 2);
        WAIT_V(16); BAR(); KD_ISSUE1(8, 3);
        WAIT_V(16); BAR(); KD_ISSUE1(9, 4);
        WAIT_V(16); BAR(); KD_ISSUE1(10, 0);
        WAIT_V(16); BAR(); KD_ISSUE1(11, 1);
        WAIT_V(16); BAR(); KD_ISSUE1(12, 2);
        WAIT_V(16); BAR(); KD_ISSUE1(13, 3);
        WAIT_V(16); BAR(); KD_YASK(); KD_ISSUE1(14, 4);
        WAIT_V(17); BAR(); KD_ISSUE1(15, 0);
        WAIT_V(17); BAR(); KD_ISSUE1(16, 1);
        WAIT_V(16); KD_YCHECK(); BAR(); KD_LDY(0); KD_ISSUE1(17, 2);
        WAIT_V(17); BAR(); KD_LDY(1); KD_ISSUE1(18, 3);
        WAIT_V(18); BAR(); KD_LDY(2); KD_ISSUE1(19, 4);
        WAIT_V(18); BAR(); KD_LDY(3); KD_ISSUE1(20, 0);
        WAIT_V(18); BAR(); KD_ISSUE1(21, 1);
        WAIT_V(17); BAR(); KD_ISSUE1(22, 2);
        WAIT_V(16); BAR(); KD_ISSUE2(23, 3);
        WAIT_V(14); BAR(); KD_ISSUE2(24, 4);
        WAIT_V(12); BAR(); KD_ISSUE2(25, 0);
        WAIT_V(12); BAR(); KD_ISSUE2(26, 1);
        WAIT_V(12); BAR(); KD_PUT(); KD_ISSUE2(27, 2); WAIT_L(0);
        WAIT_V(12); BAR(); KD_ISSUE2(28, 3);
        WAIT_V(12); BAR(); KD_ISSUE2(29, 4);
        WAIT_V(12); BAR(); KD_ISSUE2(30, 0);
        WAIT_V(12); BAR(); KD_ISSUE2(31, 1);
        WAIT_V(12); BAR(); KD_ISSUE2(32, 2);
        WAIT_V(12); BAR();
        WAIT_V(6); BAR();
        WAIT_V(0); BAR();
#undef KD_LDY
#undef KD_YASK
#undef KD_YCHECK
#undef KD_PUT
}
__device__ __forceinline__ void gemm2_mult(const Params& P, LAS unsigned char* lds, int ks, int pm, int pn, int wid, int lane, u64 tag) {
    const int tid = threadIdx.x, spare = (1 - ks) * 8192;
    const int rg = wid >> 1, wc = wid & 1, fr = lane & 15, fq = lane >> 4, q = (lane & 15) >> 2, p = lane & 3;
    const unsigned ldsb = (unsigned)(uintptr_t)lds;
    const int arow = ks * 64 + rg * 32 + fr, crow_ = (1 - ks) * 64 + rg * 32 + fr;
    const unsigned ak0 = ldsb + arow * 128 + 16 * ((0 + fq) ^ (fr & 7)), ak1 = ldsb + arow * 128 + 16 * ((4 + fq) ^ (fr & 7));
    const unsigned ck0 = ldsb + crow_ * 128 + 16 * ((0 + fq) ^ (fr & 7)) - 4096, ck1 = ldsb + crow_ * 128 + 16 * ((4 + fq) ^ (fr & 7)) - 4096;
    unsigned bo[4];
#pragma unroll
    for (int n = 0; n < 4; ++n) bo[n] = ldsb + 16384 + 256 * (8 * fq + q) + 16 * ((8 * wc + 2 * n + (p >> 1)) ^ (4 * q + 2 * (fq & 1))) + 8 * (p & 1);
#define KE_M(Af, Bf, kk, m, n) acc[m][n] = __builtin_amdgcn_mfma_f32_16x16x32_bf16(CAT(Bf[kk][n][0], Bf[kk][n][1]), Af[kk][m], acc[m][n], 0, 0, 0)
#define KE_WAITL(Af, Bf, NM) do { asm volatile("s_waitcnt lgkmcnt(0)" : "+v"(Bf[0][0][0]), "+v"(Bf[0][0][1]), "+v"(Bf[0][1][0]), "+v"(Bf[0][1][1]), "+v"(Bf[0][2][0]), "+v"(Bf[0][2][1]), "+v"(Bf[0][3][0]), "+v"(Bf[0][3][1]), \
        "+v"(Bf[1][0][0]), "+v"(Bf[1][0][1]), "+v"(Bf[1][1][0]), "+v"(Bf[1][1][1]), "+v"(Bf[1][2][0]), "+v"(Bf[1][2][1]), "+v"(Bf[1][3][0]), "+v"(Bf[1][3][1]) :: "memory"); \
        asm volatile("" : "+v"(Af[0][0]), "+v"(Af[0][1]), "+v"(Af[1][0]), "+v"(Af[1][1])); if ((NM) == 4) asm volatile("" : "+v"(Af[0][2]), "+v"(Af[0][3]), "+v"(Af[1][2]), "+v"(Af[1][3])); } while (0)
    f32x4 acc[4][4];
#pragma unroll
    for (int m = 0; m < 4; ++m)
#pragma unroll
        for (int n = 0; n < 4; ++n) acc[m][n] = (f32x4){0.f, 0.f, 0.f, 0.f};
    bf16x8 PA[2][4], QA[2][4]; bf16x4 PB[2][4][2], QB[2][4][2];
    int so = 0;
#define KD_ADDR() const unsigned _a0 = ak0 + so, _a1 = ak1 + so, _c0 = ck0 + so, _c1 = ck1 + so, _b0 = bo[0] + so, _b1 = bo[1] + so, _b2 = bo[2] + so, _b3 = bo[3] + so
#define KD_NEXT() so = (so == 4 * KB_STAGE) ? 0 : so + KB_STAGE
    BAR();
    { KD_ADDR(); KE_S01(QA, QB, PA, PB, _a0, _a1, _c0, _c1, _b0, _b1, _b2, _b3); KD_NEXT(); }
#define KE_STEP(MAC, NM, XA, XB, YA, YB) do { KD_ADDR(); unsigned _u0 = _a0, _u1 = _a1, _w0 = _c0, _w1 = _c1, _v0 = _b0, _v1 = _b1, _v2 = _b2, _v3 = _b3; \
        asm volatile("" : "+v"(_u0), "+v"(_u1), "+v"(_w0), "+v"(_w1), "+v"(_v0), "+v"(_v1), "+v"(_v2), "+v"(_v3)); \
        KE_WAITL(XA, XB, NM); BAR(); SCHED(); MAC(XA, XB, YA, YB, _u0, _u1, _w0, _w1, _v0, _v1, _v2, _v3); KD_NEXT(); } while (0)
    const int erow = pm * 128 + ks * 64 + rg * 32 + fr;
    const int ecol = pn * 128 + wc * 64 + 16 * (fq & 1) + 8 * (fq >> 1);
    for (int s = 0; s < 22; s += 2) {
        KE_STEP(KE_S11, 4, PA, PB, QA, QB);
        KE_STEP(KE_S11, 4, QA, QB, PA, PB);
    }
    KE_STEP(KE_S12, 4, PA, PB, QA, QB);
    const rsrc_t rp = mk_rsrc(P.part, 256 * 16384);
    LAS unsigned* cnt = (LAS unsigned*)(lds + 4 * KB_STAGE + spare);
    if (tid == 0) *cnt = 0u;
#pragma unroll
    for (int m = 0; m < 2; ++m)
#pragma unroll
        for (int h = 0; h < 2; ++h) {
            const f32x4 a0 = acc[2 + m][2 * h], a1 = acc[2 + m][2 * h + 1];
            st16(rp, blockIdx.x * 16384u + (unsigned)(wid * 4096 + (m * 2 + h) * 1024 + lane * 16),
                 (u32x4){pack_bf16(a0[0], a0[1]), pack_bf16(a0[2], a0[3]), pack_bf16(a1[0], a1[1]), pack_bf16(a1[2], a1[3])});
        }
    KE_STEP(KE_S22, 2, QA, QB, PA, PB);
    KE_STEP(KE_S22, 2, PA, PB, QA, QB);
    KE_STEP(KE_S22, 2, QA, QB, PA, PB);
    KE_STEP(KE_S22, 2, PA, PB, QA, QB);
    WAIT_V(0);
    if (last_wave(cnt, 4) && lane == 0) signal(P.sync + SY_P + blockIdx.x * 16, tag);
    KE_STEP(KE_S22, 2, QA, QB, PA, PB);
    KE_STEP(KE_S22, 2, PA, PB, QA, QB);
    u64 pf;
    {
        const u64* pfl = P.sync + SY_P + (blockIdx.x ^ 1) * 16;
        asm volatile("global_load_dwordx2 %0, %1, off sc1" : "=&v"(pf) : "v"(pfl) : "memory");
    }
    KE_STEP(KE_S22, 2, QA, QB, PA, PB);
    KE_STEP(KE_S22, 2, PA, PB, QA, QB);
    asm volatile("s_waitcnt vmcnt(0)" : "+v"(pf) :: "memory");
    if (!__all(pf == tag)) poll_words(P.sync + SY_P + (blockIdx.x ^ 1) * 16, true, tag);
    asm volatile("" ::: "memory");
    u32x4 pin[2][2];
#pragma unroll
    for (int m = 0; m < 2; ++m)
#pragma unroll
        for (int h = 0; h < 2; ++h) {
            const unsigned o = (blockIdx.x ^ 1) * 16384u + (unsigned)(wid * 4096 + (m * 2 + h) * 1024 + lane * 16);
            asm volatile("buffer_load_dwordx4 %0, %1, %2, 0 offen sc1" : "=&v"(pin[m][h]) : "v"(o), "s"(rp) : "memory");
        }
    KE_STEP(KE_S22, 2, QA, QB, PA, PB);
    KE_WAITL(PA, PB, 2); SCHED();
    KE_S20(PA, PB, QA, QB, 0, 0, 0, 0, 0, 0, 0, 0);
    bf16x4 yv[2][2][2]; u32x4 xv[2][2]; f32x2 st[2];
#pragma unroll
    for (int m = 0; m < 2; ++m) {
        st[m] = *(const LAS f32x2*)(lds + 4 * KB_STAGE + spare + 1024 + (ks * 64 + rg * 32 + 16 * m + fr) * 8);
#pragma unroll
        for (int h = 0; h < 2; ++h) {
            const int xr = 16 * m + fr, xc = 8 * wc + 4 * h + 2 * (fq & 1) + (fq >> 1);
            xv[m][h] = *(const LAS u32x4*)(lds + rg * KB_STAGE + spare + xr * 256 + 16 * (xc ^ (xr & 15)));
        }
    }
#pragma unroll
    for (int m = 0; m < 2; ++m)
#pragma unroll
        for (int h = 0; h < 2; ++h) {
            const int crow = 32 * h + 16 * (fq & 1) + 8 * (fq >> 1) + q;
            const int tck = (rg * 32 + 16 * m) / 8 + (p >> 1);
            const unsigned yb = ldsb + (2 + wc) * KB_STAGE + spare + crow * 128 + 8 * (p & 1);
            TRRD(yv[m][h][0], yb + 16 * (tck ^ (((crow >> 1) & 3) << 1)), 0);
            TRRD(yv[m][h][1], yb + 16 * (tck ^ ((((crow + 4) >> 1) & 3) << 1)), 512);
        }
    asm volatile("s_waitcnt vmcnt(0) lgkmcnt(0)" : "+v"(pin[0][0]), "+v"(pin[0][1]), "+v"(pin[1][0]), "+v"(pin[1][1]), "+v"(yv[0][0][0]), "+v"(yv[0][0][1]), "+v"(yv[0][1][0]), "+v"(yv[0][1][1]),
                 "+v"(yv[1][0][0]), "+v"(yv[1][0][1]), "+v"(yv[1][1][0]), "+v"(yv[1][1][1]) :: "memory");
    const rsrc_t ro = mk_rsrc(P.out, SEQ * DM * 2);
#pragma unroll
    for (int m = 0; m < 2; ++m) {
        const float rs = st[m][1], mrs = -st[m][0] * rs;
        u32x4 res[2];
#pragma unroll
        for (int h = 0; h < 2; ++h) {
            const u32x4 w = pin[m][h];
            const f32x4 a0 = acc[m][2 * h] + (f32x4){bf_lo(w[0]), bf_hi(w[0]), bf_lo(w[1]), bf_hi(w[1])};
            const f32x4 a1 = acc[m][2 * h + 1] + (f32x4){bf_lo(w[2]), bf_hi(w[2]), bf_lo(w[3]), bf_hi(w[3])};
            float o[8];
#pragma unroll
            for (int j = 0; j < 4; ++j) {
                const auto sw = __builtin_amdgcn_permlane16_swap(__float_as_uint(a0[j]), __float_as_uint(a1[j]), false, false);
                o[j] = __uint_as_float(sw[0]); o[4 + j] = __uint_as_float(sw[1]);
            }
#pragma unroll
            for (int j = 0; j < 8; ++j) {
                const unsigned xw = xv[m][h][j >> 1];
                const float xf = (j & 1) ? bf_hi(xw) : bf_lo(xw);
                const float yf = __uint_as_float((unsigned)(u16)yv[m][h][j >> 2][j & 3] << 16);
                o[j] += xf + (xf * rs + mrs) * yf;
            }
            res[h] = (u32x4){pack_bf16(o[0], o[1]), pack_bf16(o[2], o[3]), pack_bf16(o[4], o[5]), pack_bf16(o[6], o[7])};
        }
        u32x4 sa, sb;
#pragma unroll
        for (int i = 0; i < 4; ++i) {
            const unsigned y = (unsigned)__builtin_amdgcn_update_dpp(0, (int)res[1][i], 0x128  , 0xf, 0xf, false);
            sa[i] = fr < 8 ? res[0][i] : y; sb[i] = fr < 8 ? y : res[0][i];
        }
        const int rbase = erow - fr + 16 * m + (fr & 7), cbase = ecol;
        __builtin_amdgcn_raw_buffer_store_b128(sa, ro, (unsigned)((rbase * DM + cbase + (fr < 8 ? 0 : 32)) * 2), 0, 16);
        __builtin_amdgcn_raw_buffer_store_b128(sb, ro, (unsigned)(((rbase + 8) * DM + cbase + (fr < 8 ? 32 : 0)) * 2), 0, 16);
    }
#undef KE_M
#undef KE_WAITL
#undef KE_STEP
#undef KD_ADDR
#undef KD_NEXT
}

__global__ __launch_bounds__(512, 2) void fused_kernel(Params P) {
    extern __shared__ __attribute__((aligned(16))) unsigned char shm[];
    LAS unsigned char* lds = (LAS unsigned char*)shm;
    const int tid = threadIdx.x, wid = __builtin_amdgcn_readfirstlane(tid >> 6), lane = tid & 63;
    const int b = blockIdx.x, xcd = b & 7, bi = b >> 3;
    const u64 tag = __hip_atomic_load((gu64*)(P.sync + SY_EPOCH), __ATOMIC_RELAXED, __HIP_MEMORY_SCOPE_AGENT) + 1ull;
    const bool is_gemm = bi < 28;
    const int ks = xcd & 1, pm2 = 8 * ((xcd >> 1) & 1) + (bi & 7), pn2 = 4 * (xcd >> 2) + (bi >> 3);
    const int pm1 = 4 * (xcd & 1) + (bi & 3), pn1 = 7 * (xcd >> 1) + (bi >> 2), cb = xcd * 4 + (bi - 28);
    const G1Addr ga = gemm1_addr(P, pm1, is_gemm ? pn1 : 0);
    ConvPre cpre;
    {
        const int row = b * 8 + wid, cg = b & 15, rt = b >> 4;
        const u16* xr = P.x + (size_t)row * DM;
        u32x4 a, bq; u32x2 wt;
        asm volatile("global_load_dwordx4 %0, %1, off" : "=&v"(a) : "v"(xr + lane * 8) : "memory");
        asm volatile("global_load_dwordx4 %0, %1, off offset:1024" : "=&v"(bq) : "v"(xr + lane * 8) : "memory");
        asm volatile("global_load_dwordx2 %0, %1, off" : "=&v"(wt) : "v"(P.shk + (size_t)(cg * 64 + lane) * 4) : "memory");
#pragma unroll
        for (int i = 0; i < 2; ++i) {
            const int j = wid + 8 * i, kr = 128 * rt - 8 + 8 * j + (lane >> 3);
            __builtin_amdgcn_global_load_lds((const GAS unsigned*)(P.kin + (size_t)(kr < 0 ? 0 : kr) * DM + cg * 64 + (lane & 7) * 8), (LAS unsigned*)(lds + PA_K + j * 1024), 16, 0, 0);
            __builtin_amdgcn_global_load_lds((const GAS unsigned*)(P.vin + (size_t)(128 * rt + 8 * j + (lane >> 3)) * DM + cg * 64 + (lane & 7) * 8), (LAS unsigned*)(lds + PA_V + j * 1024), 16, 0, 0);
        }
        if (wid == 0) __builtin_amdgcn_global_load_lds((const GAS unsigned*)(P.kin + (size_t)(128 * rt + 120 + (lane >> 3)) * DM + cg * 64 + (lane & 7) * 8), (LAS unsigned*)(lds + PA_K + 16 * 1024), 16, 0, 0);
        WAIT_V(4);
        asm volatile("" : "+v"(a), "+v"(bq), "+v"(wt));
        float v[16];
#pragma unroll
        for (int i = 0; i < 4; ++i) { v[2 * i] = bf_lo(a[i]); v[2 * i + 1] = bf_hi(a[i]); v[8 + 2 * i] = bf_lo(bq[i]); v[9 + 2 * i] = bf_hi(bq[i]); }
        float s = 0.f;
#pragma unroll
        for (int i = 0; i < 16; ++i) s += v[i];
        const float mean = wave_sum(s) * (1.0f / DM);
        float q = 0.f;
#pragma unroll
        for (int i = 0; i < 16; ++i) { const float d = v[i] - mean; q += d * d; }
        const float mb = bf_round(mean), vb = bf_round(wave_sum(q) * (1.0f / DM));
        const float sb = bf_round(sqrtf(bf_round(vb + 1e-5f)));
        const float rs = 1.0f / sb;
        u32x4 oa, ob;
#pragma unroll
        for (int i = 0; i < 4; ++i) {
            oa[i] = pack_bf16(bf_round(v[2 * i] - mb) * rs, bf_round(v[2 * i + 1] - mb) * rs);
            ob[i] = pack_bf16(bf_round(v[8 + 2 * i] - mb) * rs, bf_round(v[9 + 2 * i] - mb) * rs);
        }
        const float w0 = bf_lo(wt[0]), w1 = bf_hi(wt[0]), w2 = bf_lo(wt[1]), w3 = bf_hi(wt[1]);
        const rsrc_t rx = mk_rsrc(P.xn, SEQ * DM * 2);
        st16(rx, (unsigned)(row * DM * 2 + lane * 16), oa); st16(rx, (unsigned)(row * DM * 2 + 1024 + lane * 16), ob);
        st8(mk_rsrc(P.stats, SEQ * 8), lane == 0 ? (unsigned)(row * 8) : 0xfffffff0u, (u32x2){__float_as_uint(mb), __float_as_uint(rs)});
        if (!is_gemm) { conv_prefetch(P, cb, cpre); WAIT_V(4); }
        else { STAGE_B(SB(0, 0), ga.cG, 0); STAGE_B(SB(0, 1), ga.cU, 0); STAGE_B(SB(1, 0), ga.cG, 1); STAGE_B(SB(1, 1), ga.cU, 1); WAIT_V(8); }
        WAIT_L(0); BAR();
        if (tid == 0) signal(P.sync + SY_X + b, tag);
        if (tid == 0) *(LAS unsigned*)(lds + PA_C + 4) = 0u;
        float kf[19], kv[16];
#pragma unroll
        for (int i = 0; i < 19; ++i) kf[i] = bf_lo((unsigned)*(const LAS u16*)(lds + PA_K + (16 * wid + 5 + i) * 128 + lane * 2));
        if (rt == 0 && wid == 0) { kf[0] = 0.f; kf[1] = 0.f; kf[2] = 0.f; }
#pragma unroll
        for (int i = 0; i < 16; ++i) {
            const float vf = bf_lo((unsigned)*(const LAS u16*)(lds + PA_V + (16 * wid + i) * 128 + lane * 2));
            kv[i] = (kf[i] * w3 + kf[i + 1] * w2 + kf[i + 2] * w1 + kf[i + 3] * w0) * vf;
        }
#pragma unroll
        for (int j = 0; j < 2; ++j) {
            u32x4 o;
#pragma unroll
            for (int i = 0; i < 4; ++i) o[i] = pack_bf16(kv[8 * j + 7 - 2 * i], kv[8 * j + 6 - 2 * i]);
            *(LAS u32x4*)(lds + PA_T + lane * 256 + 16 * ((2 * wid + j) ^ (lane & 15))) = o;
        }
        WAIT_L(0); BAR();
        const rsrc_t rk = mk_rsrc(P.kvT, SEQ * DM * 2);
#define PA_PUTK(w) do { _Pragma("unroll") for (int i = 0; i < 2; ++i) { const int ch = 8 * (w) + 4 * i + (lane >> 4), ck = lane & 15; \
            st16(rk, (unsigned)(((cg * 64 + ch) * SEQ + 128 * rt + 8 * ck) * 2), *(const LAS u32x4*)(lds + PA_T + ch * 256 + 16 * (ck ^ (ch & 15)))); } } while (0)
        if (is_gemm) {
            if (wid == 0) poll_words(P.sync + SY_X + pm1 * 32 + (lane & 31), true, tag);
            else {
                PA_PUTK(wid); if (wid == 1) PA_PUTK(0);
                WAIT_V(0);
                if (last_wave((LAS unsigned*)(lds + PA_C + 4), 7) && lane == 0) signal(P.sync + SY_K + (b & 15) * 16 + (b >> 4), tag);
            }
            WAIT_L(0); BARM();
            STAGE_A(SA(0, 0), 0, 0); STAGE_A(SA(0, 1), 1, 0); STAGE_A(SA(1, 0), 0, 1);
        } else {
            PA_PUTK(wid);
            WAIT_V(0);
            if (last_wave((LAS unsigned*)(lds + PA_C + 4), 8) && lane == 0) signal(P.sync + SY_K + (b & 15) * 16 + (b >> 4), tag);
        }
#undef PA_PUTK
    }
    const G2Load g2 = gemm2_addr(P, ks, pm2, pn2, wid & 3, lane);
    if (is_gemm) {
        gemm1_block(P, lds, ga, g2, ks, pm1, pn1, tag);
    } else {
        if (wid == 0) poll_words(P.sync + SY_K + (cb >> 1) * 16 + (lane & 15), true, tag);
        BARM();
        asm volatile("" : "+v"(cpre.nh[0]), "+v"(cpre.nh[1]), "+v"(cpre.nh[2]), "+v"(cpre.nh[3]));
        conv_block(P, lds, cb, cpre);
        WAIT_L(0); BAR();
        if (tid == 0) *(LAS unsigned*)(lds + SYNC_CNT) = 0u;
        WAIT_L(0); BAR();
        if (wid >= 4) { const int lw = wid - 4; KD_ISSUE1B(0, 0); KD_ISSUE1B(1, 1); KD_ISSUE1B(2, 2); KD_ISSUE1B(3, 3); }
    }
    if (wid >= 4 && !is_gemm) { WAIT_V(16); } else { WAIT_V(0); }
    if (last_wave((LAS unsigned*)(lds + SYNC_CNT), 8) && lane == 0) signal(is_gemm ? P.sync + SY_H + pm1 * 32 + pn1 : P.sync + SY_Y + cb, tag);
    if (b == 0 && wid == 7) {
        u64 w[4];
#pragma unroll
        for (int i = 0; i < 4; ++i) w[i] = __hip_atomic_load((gu64*)(P.sync + SY_X + i * 64 + lane), __ATOMIC_RELAXED, __HIP_MEMORY_SCOPE_AGENT);
        if (!__all(w[0] == tag && w[1] == tag && w[2] == tag && w[3] == tag)) {
#pragma unroll
            for (int i = 0; i < 4; ++i) poll_words(P.sync + SY_X + i * 64 + lane, true, tag);
        }
        if (lane == 0) signal(P.sync + SY_EPOCH, tag);
    }
    G2Epi ep;
    if (wid > 4) { poll_words(P.sync + SY_X + (pm2 >> 1) * 32 + (lane & 31), true, tag); gemm2_epi_request(P, ep, ks, pm2, pn2, wid - 4, lane); }
    if (wid == 4) {
        poll_words(P.sync + SY_H + (pm2 >> 1) * 32 + lane, lane < 28, tag);
        gemm2_epi_request(P, ep, ks, pm2, pn2, 0, lane);
    }
    WAIT_L(0); BARM();
    if (wid >= 4) {
        gemm2_loader(P, lds, g2, ep, ks, pm2, pn2, wid - 4, lane, tag);
    } else {
        gemm2_mult(P, lds, ks, pm2, pn2, wid, lane, tag);
    }
}

extern "C" void kernel_launch(void* const* d_in, const int* in_sizes, int n_in, void* d_out, int out_size, void* d_ws, size_t ws_size, hipStream_t stream) {
    unsigned char* ws = (unsigned char*)d_ws;
    const size_t MB = 1u << 20;
    Params P;
    P.x = (const u16*)d_in[0]; P.kin = (const u16*)d_in[1]; P.vin = (const u16*)d_in[2]; P.shk = (const u16*)d_in[3]; P.ssmk = (const u16*)d_in[4]; P.ssmD = (const u16*)d_in[5];
    P.wg = (const u16*)d_in[7]; P.wu = (const u16*)d_in[8]; P.wd = (const u16*)d_in[9];
    P.out = (u16*)d_out;
    P.xn = (u16*)(ws + 0 * MB); P.kvT = (u16*)(ws + 4 * MB); P.y2T = (u16*)(ws + 8 * MB); P.hbuf = (u16*)(ws + 16 * MB);
    P.stats = (f32x2*)(ws + 12 * MB); P.part = (float*)(ws + 32 * MB); P.sync = (u64*)(ws + 13 * MB);
    (void)hipFuncSetAttribute((const void*)fused_kernel, hipFuncAttributeMaxDynamicSharedMemorySize, LDS_BYTES);
    fused_kernel<<<256, 512, LDS_BYTES, stream>>>(P);
}
```
